```python
import torch

def _softmax(x, axis=-1):
    return torch.softmax(x, dim=axis)
def _gelu(x):
    return torch.nn.functional.gelu(x, approximate="tanh")
def _silu(x):
    return torch.nn.functional.silu(x)
def _sigmoid(x):
    return torch.sigmoid(x)
def _layernorm(x, axis=-1, eps=1e-5):
    m = x.mean(dim=axis, keepdim=True)
    v = x.var(dim=axis, keepdim=True, unbiased=False)
    return (x - m) / torch.sqrt(v + eps)
def _scatter_add(tgt, idx, src, axis=0):
    return tgt.clone().index_add_(axis, idx, src)
def _segment_sum(src, seg, n):
    out = torch.zeros((n,) + src.shape[1:], dtype=src.dtype)
    return out.index_add_(0, seg, src)
def _pad(x, pw):
    flat = tuple(v for pair in reversed(pw) for v in pair)
    return torch.nn.functional.pad(x, flat)

kernel_name = 'standalone__deep_cross_xdeepfm__recsys'

def setup_inputs():

    _w = (lambda a: (lambda t: t.to(torch.bfloat16) if t.is_floating_point() else t)(torch.as_tensor(a)))
    B, F, D, L = 16384, 26, 16, 3
    V = min(1000, 100000)
    Din = F * D
    return {
        "idx": _w((lambda lo, hi, *s: torch.randint(lo, hi, s))(0, V, B, F).to(torch.int64)),
        "table": _w((lambda *s: torch.randn(*s, dtype=torch.bfloat16) * 0.8)(V, D)),
        "ws_cin": _w((lambda *s: torch.randn(*s, dtype=torch.bfloat16) * 0.2)(L, F, F, F) * (1.0 / F)),
        "w_cin_out": _w((lambda *s: torch.randn(*s, dtype=torch.bfloat16) * 0.2)(L * F) * ((L * F) ** -0.5)),
        "w_dnn1": _w((lambda *s: torch.randn(*s, dtype=torch.bfloat16) * 0.8)(Din, Din) * (Din ** -0.5)),
        "b_dnn1": _w((lambda *s: torch.randn(*s, dtype=torch.bfloat16) * 3.2)(Din) * 0.1),
        "w_dnn2": _w((lambda *s: torch.randn(*s, dtype=torch.bfloat16) * 0.2)(Din) * (Din ** -0.5)),
        "b_out": _w((lambda *s: torch.randn(*s, dtype=torch.bfloat16) * 0.2)(1)),
    }

def reference(idx, table, ws_cin, w_cin_out, w_dnn1, b_dnn1, w_dnn2, b_out):
    x0 = table[idx]
    B = x0.shape[0]
    L = ws_cin.shape[0]
    xk = x0
    pooled = []
    for l in range(L):
        xk = torch.einsum('bid,bjd,hij->bhd', xk, x0, ws_cin[l])
        pooled.append(xk.sum(axis=-1))
    p_plus = torch.stack(pooled, axis=1).reshape(B, -1)
    logit_cin = p_plus @ w_cin_out
    h = x0.reshape(B, -1)
    h = _gelu(h @ w_dnn1 + b_dnn1)
    logit_dnn = h @ w_dnn2
    return logit_cin + logit_dnn + b_out
```

```cpp
#include <hip/hip_runtime.h>
#include <hip/hip_bf16.h>

__device__ __forceinline__ float bf2f(unsigned short u) { return __uint_as_float(((unsigned)u) << 16); }

__global__ __launch_bounds__(64) void naive_kernel(const int* idx, const unsigned short* table, const unsigned short* wc,
    const unsigned short* wout, const unsigned short* w1, const unsigned short* b1, const unsigned short* w2,
    const unsigned short* bout, __hip_bfloat16* out) {
  __shared__ float x0[26*16], xk[26*16], xn[26*16], red[64];
  int b = blockIdx.x, t = threadIdx.x;
  for (int i = t; i < 416; i += 64) { int f = i >> 4, d = i & 15; float v = bf2f(table[idx[b*26+f]*16+d]); x0[i] = v; xk[i] = v; }
  __syncthreads();
  float acc = 0.f;
  for (int l = 0; l < 3; ++l) {
    for (int o = t; o < 416; o += 64) {
      int h = o >> 4, d = o & 15; float s = 0.f;
      for (int i = 0; i < 26; ++i) { float xi = xk[i*16+d];
        for (int j = 0; j < 26; ++j) s += bf2f(wc[((l*26+h)*26+i)*26+j]) * xi * x0[j*16+d]; }
      xn[o] = s; acc += s * bf2f(wout[l*26+h]);
    }
    __syncthreads();
    for (int o = t; o < 416; o += 64) xk[o] = xn[o];
    __syncthreads();
  }
  for (int n = t; n < 416; n += 64) {
    float s = bf2f(b1[n]);
    for (int k = 0; k < 416; ++k) s += x0[k] * bf2f(w1[k*416+n]);
    float u = 0.7978845608028654f * (s + 0.044715f * s * s * s);
    float g = 0.5f * s * (1.f + tanhf(u));
    acc += g * bf2f(w2[n]);
  }
  red[t] = acc; __syncthreads();
  if (t == 0) { float s = bf2f(bout[0]); for (int i = 0; i < 64; ++i) s += red[i]; out[b] = __float2bfloat16(s); }
}

extern "C" void kernel_launch(void* const* d_in, const int* in_sizes, int n_in,
                              void* d_out, int out_size, void* d_ws, size_t ws_size,
                              hipStream_t stream) {
  naive_kernel<<<out_size, 64, 0, stream>>>((const int*)d_in[0], (const unsigned short*)d_in[1], (const unsigned short*)d_in[2],
     (const unsigned short*)d_in[3], (const unsigned short*)d_in[4], (const unsigned short*)d_in[5], (const unsigned short*)d_in[6],
     (const unsigned short*)d_in[7], (__hip_bfloat16*)d_out);
}
```

```cpp
#include <hip/hip_runtime.h>
#include <hip/hip_bf16.h>

static __device__ const __attribute__((aligned(128))) unsigned KTAB[784] = {0xa01e7df9,0x818b7351,0xa01f8391,0x19627df9,0xa01d0000,0x74547352,0xa01c0100,0x1f7f0000,0x7dfa861b,0x79eb79e9,0x77e374d5,0x19647dfa,0x17cf4,0x851479ea,0x10174d6,0x2ba80001,0x7cf77cf5,0x7aed8517,0x75db74d7,0x19667dfb,0x7df87cf6,0x7aee7aec,0x830c75d8,0x2ba90002,0x7eff871d,0x7bf37bf1,0x830d75d9,0x18627d79,0x8000871e,0x86187bf2,0x830e75da,0x1e7f0003,0x8001871f,0x78e678e4,0x76de7761,0x18647d7a,0x80027efc,0x78e58413,0x76dd7762,0x2aa80080,0x80037efd,0xa01ba01f,0xa01aa01e,0x18667d7b,0x88207efe,0xa01aa01e,0xa019a01d,0x2aa90081,0x7353724d,0x89258924,0x71c8714a,0xc367763,0x8188724e,0xa019a01d,0xa018a01b,0x2080082,0x8189724f,0xa017a01c,0xa017a014,0x24908390,0x818a7350,0xa016a01b,0xa016a013,0x2090083,0xa01ca01a,0x7b717559,0xa015a012,0x24928391,0xa012a013,0x7b72755a,0xa014a00e,0x1e7e0100,0xa011a012,0x869d8515,0xa013a00d,0xd3677e3,0xa010a011,0x869e8516,0xa012a00c,0x3080101,0xa00f7dfb,0x869f7aef,0xa0108410,0x25908410,0xa00e0002,0x7e7c7bf0,0xa00f0102,0x3090102,0x7d798821,0x7e7d8619,0x8411830f,0x25928411,0x38822,0x7e7e861a,0x10376dc,0x1f7e0103,0x859b8823,0x839379e8,0x77e277e0,0x24948392,0x7c748104,0x786478e7,0x77e176df,0x25940180,0x7c758105,0xa015a018,0xa00ba00b,0x5da4a01a,0x7c768106,0xa010a014,0xa009a008,0x5da6a01b,0x79697455,0x88a48926,0x71ca71c9,0x5da8a01c,0x796a7456,0xa00fa00e,0xa007a007,0x5daaa01d,0x84977457,0xa00ca00d,0xa006a006,0x5daca01e,0x7a6c7558,0xa00aa00b,0xa005a005,0x5daea01f,0xa01fa01f,0x85997b73,0xa01fa01f,0x19827e09,0xa01ea01e,0x859a8598,0xa01ea01e,0x1f9f0010,0xa01da01d,0x87a17e7f,0xa01da01d,0x19847e0a,0xa01ca01c,0x87a27f80,0xa01ca01c,0x2bc80011,0xa01aa019,0x87a37f81,0xa01ba01b,0x19867e0b,0xa019a016,0x80847f82,0xa01aa01a,0x2bc90012,0xa018a015,0x80857f83,0xa019a019,0x18827d89,0xa017a013,0x808687a0,0xa018a018,0x1e9f0013,0xa0147d7a,0x78677865,0xa0178392,0x18847d8a,0xa0130080,0x79687866,0xa0160180,0x2ac80090,0x7d7b7c77,0xa01ea01c,0xa015a017,0x18867d8b,0x817d78,0xa01da01b,0xa014a016,0x2ac90091,0x8495796b,0x88a688a5,0xa013714b,0xc567773,0x84968494,0xa01ba01a,0xa012a015,0x2280092,0x7a6f7a6d,0xa018a019,0xa011a014,0x24b083a0,0x7b707a6e,0xa017a017,0xa010a013,0x2290093,0xa012a012,0x828f828d,0xa00fa011,0x24b283a1,0xa011a011,0x765c828e,0xa00ea010,0x1e9e0110,0xa00ea010,0x73d372cd,0xa00da00f,0xd5677f3,0xa00da00f,0x820872ce,0xa00ca00e,0x3280111,0xa00ca00a,0x820972cf,0xa00ba00d,0x25b08420,0xa00ba009,0x820a73d0,0xa00aa00c,0x3290112,0xa00aa008,0x820b73d1,0xa009a00a,0x25b28421,0xa009a007,0x74d473d2,0xa008a009,0x1f9e0113,0xa008a006,0x765f765d,0xa007a008,0x24b483a2,0xa007a005,0x7760765e,0xa006a007,0x25b40190,0xa006a004,0xa016a016,0xa005a006,0x5dc4a02a,0xa005a001,0xa015a015,0xa004a005,0x5dc6a02b,0xa0047763,0x71497148,0xa003a004,0x5dc8a02c,0xa0020082,0xa012a014,0xa002a003,0x5dcaa02d,0x8390755b,0xa011a00c,0xa001a002,0x5dcca02e,0x83828c,0xa010a00b,0xa000a001,0x5dcea02f,0xa01aa01e,0x724fa01f,0xa01e8390,0x19627df9,0xa018a01d,0x7350a01e,0xa01d0083,0x1f7f0000,0x7df9a01c,0x7351a01d,0x8391828f,0x19647dfa,0xa01b,0x7352a01c,0x100765c,0x2ba80001,0x861ba019,0x79e9a01b,0x74d573d3,0x19667dfb,0x7cf4a016,0x79eaa01a,0x74d68208,0x2ba90002,0x7cf5a015,0x8517a019,0x74d78209,0x18627d79,0x7cf6a014,0x7aeca017,0x75d8820a,0x1e7f0003,0x871da012,0x7bf1a016,0x75d9820b,0x18647d7a,0x871ea011,0x7bf2a015,0x75da74d4,0x2aa80080,0x871fa010,0x78e4a012,0x7761765f,0x18667d7b,0x7efca00f,0x8413a011,0x77627760,0x2aa90081,0x7efda00e,0x8924a010,0x714a7149,0xc367763,0x7efea00d,0xa01fa00f,0xa01ba01a,0x2080082,0x724da00c,0xa01ea00c,0xa014a019,0x24908390,0x724ea00b,0xa01da008,0xa013a018,0x2090083,0xa017a00a,0x74578189,0xa012a017,0x24928391,0xa013a009,0x7558818a,0xa00ea016,0x1e7e0100,0xa012a007,0x7559818b,0xa00da015,0xd3677e3,0xa011a006,0x755a7454,0xa00ca012,0x3080101,0xa0107dfa,0x851579eb,0xa00b77e3,0x25908410,0xa00f0001,0x85168514,0xa0080101,0x3090102,0x7dfb7cf7,0x7aef7aed,0x841075db,0x25928411,0x27df8,0x7bf07aee,0x102830c,0x1f7e0103,0x88217eff,0x86197bf3,0x830f830d,0x24948392,0x88228000,0x861a8618,0x76dc830e,0x25940180,0x88238001,0x79e878e6,0x77e076de,0x5da4a01a,0x81048002,0x78e778e5,0x76df76dd,0x5da6a01b,0x81058003,0x89268925,0x71c971c8,0x5da8a01c,0x81068820,0xa01ca007,0xa007a011,0x5daaa01d,0x74557353,0xa01ba004,0xa006a007,0x5daca01e,0x74568188,0xa014a003,0xa005a006,0x5daea01f,0xa01fa01f,0x7a6d8497,0xa01fa01f,0x19827e09,0xa01ea01e,0x7a6e7a6c,0xa01ea01e,0x1f9f0010,0xa01da01d,0x7b737b71,0xa01da01d,0x19847e0a,0xa01ca01c,0x85987b72,0xa01ca01c,0x2bc80011,0xa01ba01a,0x7e7f869d,0xa01ba01b,0x19867e0b,0xa019a018,0x7f80869e,0xa01aa01a,0x2bc90012,0xa016a017,0x7f81869f,0xa019a019,0x18827d89,0xa015a013,0x7f827e7c,0xa018a018,0x1e9f0013,0xa0137d79,0x7f837e7d,0xa0178411,0x18847d8a,0xa0120003,0x87a07e7e,0xa0160103,0x2ac80090,0x7d7a859b,0x78658393,0x839277e2,0x18867d8b,0x807c74,0x78667864,0x18077e1,0x2ac90091,0x7c777c75,0x88a588a4,0x714b71ca,0xc567773,0x7d787c76,0xa01ca01b,0xa015a017,0x2280092,0x796b7969,0xa01aa018,0xa014a016,0x24b083a0,0x8494796a,0xa019a016,0xa013a015,0x2290093,0xa011a012,0x755b7a6f,0xa011a014,0x24b283a1,0xa010a011,0x828c7b70,0xa010a013,0x1e9e0110,0xa00fa010,0x828d8599,0xa00fa012,0xd5677f3,0xa00ea00f,0x828e859a,0xa00ea010,0x3280111,0xa00da00e,0x72cd87a1,0xa00da00f,0x25b08420,0xa00ca00d,0x72ce87a2,0xa00ca00e,0x3290112,0xa00aa00c,0x72cf87a3,0xa00aa00d,0x25b28421,0xa009a00b,0x73d08084,0xa009a00c,0x1f9e0113,0xa008a008,0x73d18085,0xa008a00b,0x24b483a2,0xa007a007,0x73d28086,0xa007a009,0x25b40190,0xa006a006,0x765d7867,0xa006a008,0x5dc4a02a,0xa005a005,0x765e7968,0xa005a007,0x5dc6a02b,0xa0047d7b,0x714888a6,0xa004a006,0x5dc8a02c,0xa0010081,0xa017a015,0xa003a005,0x5dcaa02d,0x77638495,0xa016a014,0xa002a004,0x5dcca02e,0x828496,0xa015a00e,0xa001a000,0x5dcea02f,0xa01e7df9,0x818b7351,0xa01f8391,0x19627df9,0xa01d0000,0x74547352,0xa01c0100,0x1f7f0000,0x7dfa861b,0x79eb79e9,0x77e374d5,0x19647dfa,0x17cf4,0x851479ea,0x10174d6,0x2ba80001,0x7cf77cf5,0x7aed8517,0x75db74d7,0x19667dfb,0x7df87cf6,0x7aee7aec,0x830c75d8,0x2ba90002,0x7eff871d,0x7bf37bf1,0x830d75d9,0x18627d79,0x8000871e,0x86187bf2,0x830e75da,0x1e7f0003,0x8001871f,0x78e678e4,0x76de7761,0x18647d7a,0x80027efc,0x78e58413,0x76dd7762,0x2aa80080,0x80037efd,0xa01ba01f,0xa01aa01e,0x18667d7b,0x88207efe,0xa01aa01e,0xa019a01d,0x2aa90081,0x7353724d,0xa019a01d,0xa018a01b,0xc367763,0x8188724e,0xa017a01c,0xa017a014,0x2080082,0x8189724f,0xa016a01b,0xa016a013,0x24908390,0x818a7350,0xa015a018,0xa015a012,0x2090083,0xa01ca01a,0x7b717559,0xa014a00e,0x24928391,0xa012a013,0x7b72755a,0xa013a00d,0x1e7e0100,0xa011a012,0x869d8515,0xa012a00c,0xd3677e3,0xa010a011,0x869e8516,0xa010a00b,0x3080101,0xa00f7dfb,0x869f7aef,0xa00f8410,0x25908410,0xa00e0002,0x7e7c7bf0,0xa00b0102,0x3090102,0x7d798821,0x7e7d8619,0x8411830f,0x25928411,0x38822,0x7e7e861a,0x10376dc,0x1f7e0103,0x859b8823,0x839379e8,0x77e277e0,0x24948392,0x7c748104,0x786478e7,0x77e176df,0x25940180,0x7c758105,0xa010a014,0xa00aa00a,0x5da4a01a,0x7c768106,0xa00fa00e,0xa009a009,0x5da6a01b,0x79697455,0xa00ca00d,0xa008a008,0x5da8a01c,0x796a7456,0xa00aa00b,0xa007a007,0x5daaa01d,0x84977457,0xa009a006,0xa006a006,0x5daca01e,0x7a6c7558,0xa008a005,0xa005a005,0x5daea01f,0xa01fa01f,0x85997b73,0xa01fa01f,0x19827e09,0xa01ea01e,0x859a8598,0xa01ea01e,0x1f9f0010,0xa01da01d,0x87a17e7f,0xa01da01d,0x19847e0a,0xa01ca01c,0x87a27f80,0xa01ca01c,0x2bc80011,0xa01aa019,0x87a37f81,0xa01ba01b,0x19867e0b,0xa019a016,0x80847f82,0xa01aa01a,0x2bc90012,0xa018a015,0x80857f83,0xa019a019,0x18827d89,0xa017a013,0x808687a0,0xa018a018,0x1e9f0013,0xa0147d7a,0x78677865,0xa0178392,0x18847d8a,0xa0130080,0x79687866,0xa0160180,0x2ac80090,0x7d7b7c77,0xa01ea01c,0xa015a017,0x18867d8b,0x817d78,0xa01da01b,0xa014a016,0x2ac90091,0x8495796b,0xa01ba01a,0xa013a015,0xc567773,0x84968494,0xa018a019,0xa012a014,0x2280092,0x7a6f7a6d,0xa017a017,0xa011a013,0x24b083a0,0x7b707a6e,0xa016a016,0xa010a011,0x2290093,0xa012a012,0x828f828d,0xa00fa010,0x24b283a1,0xa011a011,0x765c828e,0xa00ea00f,0x1e9e0110,0xa00ea010,0x73d372cd,0xa00da00e,0xd5677f3,0xa00da00f,0x820872ce,0xa00ca00d,0x3280111,0xa00ca00a,0x820972cf,0xa00ba00c,0x25b08420,0xa00ba009,0x820a73d0,0xa00aa00b,0x3290112,0xa00aa008,0x820b73d1,0xa009a00a,0x25b28421,0xa009a007,0x74d473d2,0xa008a009,0x1f9e0113,0xa008a006,0x765f765d,0xa007a008,0x24b483a2,0xa007a005,0x7760765e,0xa006a007,0x25b40190,0xa006a004,0xa015a015,0xa005a006,0x5dc4a02a,0xa005a001,0xa012a014,0xa004a005,0x5dc6a02b,0xa0047763,0xa011a00c,0xa003a004,0x5dc8a02c,0xa0020082,0xa010a00b,0xa002a003,0x5dcaa02d,0x8390755b,0xa00ea00a,0xa001a002,0x5dcca02e,0x83828c,0xa00da009,0xa000a001,0x5dcea02f,0x28184030,0x58481000,0x2c1c4434,0x5c4c1404,0x24206038,0x44503408,0x2c24643c,0x1c543c0c,0x10005848,0x28184030,0x14045c4c,0x2c1c4434,0xc086450,0x5c204c38,0x140c6054,0x424543c};
__global__ __launch_bounds__(512, 2) void xdeepfm_kernel(const void* idx, const void* table, const void* wc, const void* wout,
    const void* w1, const void* b1, const void* w2, const void* bo, void* out, void* ws) {
  __shared__ __attribute__((aligned(16))) char lds[163840];
  asm volatile(
    "s_load_dwordx2 s[16:17], %0, 0x10\n"
    "s_load_dwordx2 s[8:9], %0, 0x0\n"
    "s_load_dwordx2 s[20:21], %0, 0x18\n"
    "s_load_dwordx2 s[12:13], %0, 0x8\n"
    "s_load_dwordx2 s[24:25], %0, 0x20\n"
    "s_load_dwordx2 s[28:29], %0, 0x28\n"
    "s_load_dwordx2 s[36:37], %0, 0x30\n"
    "s_load_dwordx2 s[40:41], %0, 0x38\n"
    "s_load_dwordx2 s[34:35], %0, 0x40\n"
    "v_lshrrev_b32 v1, 6, %3\n"
    "v_and_b32 v183, 63, %3\n"
    "s_mov_b32 s49, %1\n"
    "v_readfirstlane_b32 s48, v1\n"
    "s_mov_b32 s55, 0\n"
    "s_mov_b32 s57, 8\n"
    "s_mov_b32 s46, 16\n"
    "s_mov_b32 s47, 0\n"
    "Lagain:\n"
    "v_lshlrev_b32 v2, 4, v183\n"
    "s_mov_b32 s18, 105456\n"
    "v_lshrrev_b32 v28, 4, v183\n"
    "s_mov_b32 s19, 0x20000\n"
    "v_and_b32 v27, 15, v183\n"
    "s_and_b32 s73, s48, 1\n"
    "v_bfe_u32 v30, v183, 2, 2\n"
    "s_and_b32 s65, s48, 3\n"
    "v_and_b32 v31, 3, v183\n"
    "s_cmp_ge_u32 s48, 4\n"
    "s_cselect_b32 s53, 2, s73\n"
    "v_and_b32 v26, 31, v183\n"
    "s_lshl_b32 s53, s53, 10\n"
    "v_lshrrev_b32 v29, 5, v183\n"
    "v_add_u32 v1, s53, v2\n"
    "s_mov_b32 s14, 32000\n"
    "s_mov_b32 s15, 0x20000\n"
    "v_and_b32 v41, 1, v183\n"
    "v_lshlrev_b32 v34, 2, v183\n"
    "s_cmp_lt_u32 s48, 4\n"
    "s_cbranch_scc1 Lxx\n"
    "v_and_b32 v3, 7, v183\n"
    "s_mul_i32 s51, s49, 6656\n"
    "v_lshlrev_b32 v3, 3, v3\n"
    "s_mul_i32 s54, s65, 1664\n"
    "global_load_dwordx2 v[4:5], v3, %2 offset:3072\n"
    "s_add_u32 s10, s51, 6656\n"
    "s_mov_b32 s11, 0x20000\n"
    "s_add_u32 s51, s51, s54\n"
    "v_add_u32 v3, 35152, v2\n"
    "global_load_dwordx4 v[108:111], v1, %2\n"
    "s_mul_i32 s52, s65, 7168\n"
    "s_add_u32 m0, s54, 2048\n"
    "s_waitcnt lgkmcnt(0)\n"
    "buffer_load_dwordx4 v2, s[8:11], s51 offen lds\n"
    "s_mov_b32 exec_hi, 0xff\n"
    "buffer_load_dwordx4 v2, s[8:11], s51 offen offset:1024 lds\n"
    "s_add_u32 m0, s52, 45696\n"
    "s_mov_b32 exec_hi, -1\n"
    "v_lshlrev_b32 v40, 4, v41\n"
    "buffer_load_dwordx4 v3, s[16:19], s52 offen offset:0 lds\n"
    "v_lshlrev_b32 v39, 3, v28\n"
    "buffer_load_dwordx4 v3, s[16:19], s52 offen offset:1024 lds\n"
    "v_sub_u32 v39, v27, v39\n"
    "buffer_load_dwordx4 v3, s[16:19], s52 offen offset:2048 lds\n"
    "s_add_i32 s53, s57, 127\n"
    "buffer_load_dwordx4 v3, s[16:19], s52 offen offset:3072 lds\n"
    "s_add_u32 s52, s52, 4096\n"
    "s_add_u32 m0, s52, 45696\n"
    "s_lshl_b32 s53, s53, 7\n"
    "buffer_load_dwordx4 v3, s[16:19], s52 offen offset:0 lds\n"
    "v_ashrrev_i32 v39, 1, v39\n"
    "buffer_load_dwordx4 v3, s[16:19], s52 offen offset:1024 lds\n"
    "v_lshlrev_b32_e64 v43, v40, s53\n"
    "buffer_load_dwordx4 v3, s[16:19], s52 offen offset:2048 lds\n"
    "v_cmp_eq_u32 s[88:89], 0, v39\n"
    "buffer_load_dwordx4 v3, s[16:19], s52 offen offset:3072 lds\n"
    "v_cmp_eq_u32 s[90:91], 1, v39\n"
    "v_cmp_eq_u32 s[92:93], 2, v39\n"
    "v_cmp_eq_u32 s[94:95], 3, v39\n"
    "v_cndmask_b32 v148, 0, v43, s[88:89]\n"
    "v_cndmask_b32 v149, 0, v43, s[90:91]\n"
    "v_cndmask_b32 v150, 0, v43, s[92:93]\n"
    "v_cndmask_b32 v151, 0, v43, s[94:95]\n"
    "v_cmp_eq_u32 s[88:89], -8, v39\n"
    "v_cmp_eq_u32 s[90:91], -7, v39\n"
    "v_cmp_eq_u32 s[92:93], -6, v39\n"
    "v_cmp_eq_u32 s[94:95], -5, v39\n"
    "v_cndmask_b32 v152, 0, v43, s[88:89]\n"
    "v_cndmask_b32 v153, 0, v43, s[90:91]\n"
    "v_cndmask_b32 v154, 0, v43, s[92:93]\n"
    "v_cndmask_b32 v155, 0, v43, s[94:95]\n"
    "v_mul_u32_u24 v38, 52, v27\n"
    "v_lshl_add_u32 v38, v28, 4, v38\n"
    "s_mul_i32 s74, s65, 1352\n"
    "v_lshrrev_b32 v39, 3, v183\n"
    "v_mul_u32_u24 v39, 104, v39\n"
    "s_add_u32 s53, s54, 2048\n"
    "v_add_u32 v39, s53, v39\n"
    "s_lshl_b32 s53, s65, 11\n"
    "v_add_u32 v37, s53, v2\n"
    "v_lshlrev_b32 v32, 3, v31\n"
    "s_mul_i32 s53, s65, 3072\n"
    "s_add_u32 s53, s53, 20480\n"
    "v_add_u32 v33, s53, v2\n"
    "v_add_u32 v42, 80848, v33\n"
    "s_movk_i32 s75, 4096\n"
    "s_barrier\n"
    "buffer_load_dwordx4 v[48:51], v38, s[16:19], s74 offen offset:0\n"
    "buffer_load_dwordx4 v[52:55], v38, s[16:19], s74 offen offset:832\n"
    "s_add_u32 s74, s74, 5408\n"
    "s_waitcnt vmcnt(13)\n"
    "v_add_u32_sdwa v156, v39, v4 dst_sel:DWORD dst_unused:UNUSED_PAD src0_sel:DWORD src1_sel:BYTE_0\n"
    "v_add_u32_sdwa v157, v39, v4 dst_sel:DWORD dst_unused:UNUSED_PAD src0_sel:DWORD src1_sel:BYTE_1\n"
    "v_add_u32_sdwa v158, v39, v4 dst_sel:DWORD dst_unused:UNUSED_PAD src0_sel:DWORD src1_sel:BYTE_2\n"
    "v_add_u32_sdwa v159, v39, v4 dst_sel:DWORD dst_unused:UNUSED_PAD src0_sel:DWORD src1_sel:BYTE_3\n"
    "v_add_u32_sdwa v160, v39, v5 dst_sel:DWORD dst_unused:UNUSED_PAD src0_sel:DWORD src1_sel:BYTE_0\n"
    "v_add_u32_sdwa v161, v39, v5 dst_sel:DWORD dst_unused:UNUSED_PAD src0_sel:DWORD src1_sel:BYTE_1\n"
    "v_add_u32_sdwa v162, v39, v5 dst_sel:DWORD dst_unused:UNUSED_PAD src0_sel:DWORD src1_sel:BYTE_2\n"
    "v_add_u32_sdwa v163, v39, v5 dst_sel:DWORD dst_unused:UNUSED_PAD src0_sel:DWORD src1_sel:BYTE_3\n"
    "s_waitcnt vmcnt(10)\n"
    "ds_read_b32 v164, v156 offset:0\n"
    "ds_read_b32 v165, v157 offset:0\n"
    "ds_read_b32 v166, v158 offset:0\n"
    "ds_read_b32 v167, v159 offset:0\n"
    "ds_read_b32 v168, v160 offset:0\n"
    "ds_read_b32 v169, v161 offset:0\n"
    "ds_read_b32 v170, v162 offset:0\n"
    "ds_read_b32 v171, v163 offset:0\n"
    "ds_read_b32 v172, v156 offset:832\n"
    "ds_read_b32 v173, v157 offset:832\n"
    "ds_read_b32 v174, v158 offset:832\n"
    "ds_read_b32 v175, v159 offset:832\n"
    "ds_read_b32 v176, v160 offset:832\n"
    "ds_read_b32 v177, v161 offset:832\n"
    "ds_read_b32 v178, v162 offset:832\n"
    "ds_read_b32 v179, v163 offset:832\n"
    "buffer_load_dwordx4 v[56:59], v38, s[16:19], s74 offen offset:0\n"
    "buffer_load_dwordx4 v[60:63], v38, s[16:19], s74 offen offset:832\n"
    "s_add_u32 s74, s74, 5408\n"
    "s_lshl_b32 s53, s65, 4\n"
    "s_add_u32 s53, s53, 23920\n"
    "v_mad_u32_u16 v136, v108, 4, s53\n"
    "v_mad_u32_u16 v137, v108, 4, s53 op_sel:[1,0,0,0]\n"
    "v_mad_u32_u16 v138, v109, 4, s53\n"
    "v_mad_u32_u16 v139, v109, 4, s53 op_sel:[1,0,0,0]\n"
    "v_mad_u32_u16 v140, v110, 4, s53\n"
    "v_mad_u32_u16 v141, v110, 4, s53 op_sel:[1,0,0,0]\n"
    "v_lshl_add_u32 v39, v41, 1, s53\n"
    "s_add_u32 s53, s53, 116000\n"
    "v_mad_u32_u16 v146, v111, 4, v39\n"
    "v_mad_u32_u16 v142, v111, 2, s53 op_sel:[1,0,0,0]\n"
    "s_waitcnt lgkmcnt(9)\n"
    "v_lshlrev_b32 v170, 5, v170\n"
    "v_lshlrev_b32 v168, 5, v168\n"
    "v_lshlrev_b32 v166, 5, v166\n"
    "v_lshlrev_b32 v164, 5, v164\n"
    "v_lshl_or_b32 v184, v165, 21, v164\n"
    "v_lshl_or_b32 v185, v167, 21, v166\n"
    "v_lshl_or_b32 v186, v169, 21, v168\n"
    "s_waitcnt lgkmcnt(8)\n"
    "v_lshl_or_b32 v187, v171, 21, v170\n"
    "ds_write_b128 v37, v[184:187] offset:32768\n"
    "s_waitcnt lgkmcnt(2)\n"
    "v_lshlrev_b32 v178, 5, v178\n"
    "v_lshlrev_b32 v176, 5, v176\n"
    "v_lshlrev_b32 v174, 5, v174\n"
    "v_lshlrev_b32 v172, 5, v172\n"
    "v_lshl_or_b32 v188, v173, 21, v172\n"
    "v_lshl_or_b32 v189, v175, 21, v174\n"
    "v_lshl_or_b32 v190, v177, 21, v176\n"
    "s_waitcnt lgkmcnt(1)\n"
    "v_lshl_or_b32 v191, v179, 21, v178\n"
    "ds_write_b128 v37, v[188:191] offset:33792\n"
    "buffer_load_dwordx4 v[64:67], v38, s[16:19], s74 offen offset:0\n"
    "buffer_load_dwordx4 v[68:71], v38, s[16:19], s74 offen offset:832\n"
    "s_add_u32 s74, s74, 5408\n"
    "s_waitcnt lgkmcnt(0)\n"
    "s_waitcnt vmcnt(6)\n"
    "s_barrier\n"
    "buffer_load_dwordx4 v[72:75], v38, s[16:19], s74 offen offset:0\n"
    "buffer_load_dwordx4 v[76:79], v38, s[16:19], s74 offen offset:832\n"
    "s_add_u32 s74, s74, 5408\n"
    "s_waitcnt vmcnt(7)\n"
    "v_mfma_f32_16x16x32_bf16 v[208:211], v[48:51], v[148:151], 0\n"
    "v_mfma_f32_16x16x32_bf16 v[212:215], v[48:51], v[152:155], 0\n"
    "s_waitcnt vmcnt(6)\n"
    "v_mfma_f32_16x16x32_bf16 v[216:219], v[52:55], v[152:155], 0\n"
    "v_mfma_f32_16x16x32_bf16 v[208:211], v[148:151], v[48:51], v[208:211]\n"
    "v_mfma_f32_16x16x32_bf16 v[212:215], v[148:151], v[52:55], v[212:215]\n"
    "v_mfma_f32_16x16x32_bf16 v[216:219], v[152:155], v[52:55], v[216:219]\n"
    "buffer_load_dwordx4 v[80:83], v38, s[16:19], s74 offen offset:0\n"
    "buffer_load_dwordx4 v[84:87], v38, s[16:19], s74 offen offset:832\n"
    "s_add_u32 s74, s74, 5408\n"
    "s_waitcnt vmcnt(7)\n"
    "v_mfma_f32_16x16x32_bf16 v[220:223], v[56:59], v[148:151], 0\n"
    "v_mfma_f32_16x16x32_bf16 v[224:227], v[56:59], v[152:155], 0\n"
    "s_waitcnt vmcnt(6)\n"
    "v_mfma_f32_16x16x32_bf16 v[228:231], v[60:63], v[152:155], 0\n"
    "v_mfma_f32_16x16x32_bf16 v[220:223], v[148:151], v[56:59], v[220:223]\n"
    "v_mfma_f32_16x16x32_bf16 v[224:227], v[148:151], v[60:63], v[224:227]\n"
    "v_mfma_f32_16x16x32_bf16 v[228:231], v[152:155], v[60:63], v[228:231]\n"
    "buffer_load_dwordx4 v[88:91], v38, s[16:19], s74 offen offset:0\n"
    "buffer_load_dwordx4 v[92:95], v38, s[16:19], s74 offen offset:832\n"
    "s_add_u32 s74, s74, 5408\n"
    "s_waitcnt vmcnt(7)\n"
    "v_mfma_f32_16x16x32_bf16 v[232:235], v[64:67], v[148:151], 0\n"
    "v_cvt_pk_f16_f32 v104, v208, v209\n"
    "v_cvt_pk_f16_f32 v105, v220, v221\n"
    "ds_write2_b32 v136, v104, v105 offset0:0 offset1:16\n"
    "v_mfma_f32_16x16x32_bf16 v[236:239], v[64:67], v[152:155], 0\n"
    "s_waitcnt vmcnt(6)\n"
    "v_mfma_f32_16x16x32_bf16 v[240:243], v[68:71], v[152:155], 0\n"
    "buffer_load_dwordx4 v[96:99], v38, s[16:19], s74 offen offset:0\n"
    "v_cvt_pk_f16_f32 v106, v210, v211\n"
    "v_cvt_pk_f16_f32 v107, v222, v223\n"
    "ds_write2_b32 v137, v106, v107 offset0:0 offset1:16\n"
    "v_mfma_f32_16x16x32_bf16 v[232:235], v[148:151], v[64:67], v[232:235]\n"
    "v_mfma_f32_16x16x32_bf16 v[236:239], v[148:151], v[68:71], v[236:239]\n"
    "buffer_load_dwordx4 v[100:103], v38, s[16:19], s74 offen offset:832\n"
    "v_cvt_pk_f16_f32 v104, v212, v213\n"
    "v_cvt_pk_f16_f32 v105, v224, v225\n"
    "ds_write2_b32 v138, v104, v105 offset0:0 offset1:16\n"
    "v_mfma_f32_16x16x32_bf16 v[240:243], v[152:155], v[68:71], v[240:243]\n"
    "s_waitcnt vmcnt(7)\n"
    "v_mfma_f32_16x16x32_bf16 v[244:247], v[72:75], v[148:151], 0\n"
    "s_add_u32 s74, s74, 5408\n"
    "v_cvt_pk_f16_f32 v106, v214, v215\n"
    "v_cvt_pk_f16_f32 v107, v226, v227\n"
    "ds_write2_b32 v139, v106, v107 offset0:0 offset1:16\n"
    "v_mfma_f32_16x16x32_bf16 v[248:251], v[72:75], v[152:155], 0\n"
    "s_waitcnt vmcnt(6)\n"
    "v_mfma_f32_16x16x32_bf16 v[252:255], v[76:79], v[152:155], 0\n"
    "buffer_load_dwordx4 v[8:11], v33, s[12:15], 0 offen offset:0\n"
    "v_cvt_pk_f16_f32 v104, v216, v217\n"
    "v_cvt_pk_f16_f32 v105, v228, v229\n"
    "ds_write2_b32 v140, v104, v105 offset0:0 offset1:16\n"
    "v_mfma_f32_16x16x32_bf16 v[244:247], v[148:151], v[72:75], v[244:247]\n"
    "v_mfma_f32_16x16x32_bf16 v[248:251], v[148:151], v[76:79], v[248:251]\n"
    "buffer_load_dwordx4 v[12:15], v33, s[12:15], 0 offen offset:1024\n"
    "v_cvt_pk_f16_f32 v106, v218, v219\n"
    "v_cvt_pk_f16_f32 v107, v230, v231\n"
    "ds_write2_b32 v141, v106, v107 offset0:0 offset1:16\n"
    "v_mfma_f32_16x16x32_bf16 v[252:255], v[152:155], v[76:79], v[252:255]\n"
    "buffer_load_dwordx4 v[16:19], v33, s[12:15], 0 offen offset:2048\n"
    "ds_read_u16 v128, v146 offset:0\n"
    "s_waitcnt vmcnt(8)\n"
    "v_mfma_f32_16x16x32_bf16 v[208:211], v[80:83], v[148:151], 0\n"
    "v_cvt_pk_f16_f32 v104, v232, v233\n"
    "v_cvt_pk_f16_f32 v105, v244, v245\n"
    "ds_write2_b32 v136, v104, v105 offset0:32 offset1:48\n"
    "v_mfma_f32_16x16x32_bf16 v[212:215], v[80:83], v[152:155], 0\n"
    "s_waitcnt vmcnt(7)\n"
    "v_mfma_f32_16x16x32_bf16 v[216:219], v[84:87], v[152:155], 0\n"
    "s_waitcnt lgkmcnt(1)\n"
    "v_mul_f16 v128, 0.5, v128\n"
    "v_cvt_pk_f16_f32 v106, v234, v235\n"
    "v_cvt_pk_f16_f32 v107, v246, v247\n"
    "ds_write2_b32 v137, v106, v107 offset0:32 offset1:48\n"
    "v_mfma_f32_16x16x32_bf16 v[208:211], v[148:151], v[80:83], v[208:211]\n"
    "v_mfma_f32_16x16x32_bf16 v[212:215], v[148:151], v[84:87], v[212:215]\n"
    "ds_write_b16 v142, v128 offset:0\n"
    "v_cvt_pk_f16_f32 v104, v236, v237\n"
    "v_cvt_pk_f16_f32 v105, v248, v249\n"
    "ds_write2_b32 v138, v104, v105 offset0:32 offset1:48\n"
    "v_mfma_f32_16x16x32_bf16 v[216:219], v[152:155], v[84:87], v[216:219]\n"
    "s_waitcnt vmcnt(6)\n"
    "v_mfma_f32_16x16x32_bf16 v[220:223], v[88:91], v[148:151], 0\n"
    "v_cvt_pk_f16_f32 v106, v238, v239\n"
    "v_cvt_pk_f16_f32 v107, v250, v251\n"
    "ds_write2_b32 v139, v106, v107 offset0:32 offset1:48\n"
    "v_mfma_f32_16x16x32_bf16 v[224:227], v[88:91], v[152:155], 0\n"
    "s_waitcnt vmcnt(5)\n"
    "v_mfma_f32_16x16x32_bf16 v[228:231], v[92:95], v[152:155], 0\n"
    "v_cvt_pk_f16_f32 v104, v240, v241\n"
    "v_cvt_pk_f16_f32 v105, v252, v253\n"
    "ds_write2_b32 v140, v104, v105 offset0:32 offset1:48\n"
    "v_mfma_f32_16x16x32_bf16 v[220:223], v[148:151], v[88:91], v[220:223]\n"
    "v_mfma_f32_16x16x32_bf16 v[224:227], v[148:151], v[92:95], v[224:227]\n"
    "v_cvt_pk_f16_f32 v106, v242, v243\n"
    "v_cvt_pk_f16_f32 v107, v254, v255\n"
    "ds_write2_b32 v141, v106, v107 offset0:32 offset1:48\n"
    "v_mfma_f32_16x16x32_bf16 v[228:231], v[152:155], v[92:95], v[228:231]\n"
    "ds_read_u16 v129, v146 offset:128\n"
    "s_waitcnt vmcnt(4)\n"
    "v_mfma_f32_16x16x32_bf16 v[232:235], v[96:99], v[148:151], 0\n"
    "v_cvt_pk_f16_f32 v104, v208, v209\n"
    "v_cvt_pk_f16_f32 v105, v220, v221\n"
    "ds_write2_b32 v136, v104, v105 offset0:64 offset1:80\n"
    "v_mfma_f32_16x16x32_bf16 v[236:239], v[96:99], v[152:155], 0\n"
    "s_waitcnt lgkmcnt(1)\n"
    "v_mul_f16 v129, 0.5, v129\n"
    "v_cvt_pk_f16_f32 v106, v210, v211\n"
    "v_cvt_pk_f16_f32 v107, v222, v223\n"
    "ds_write2_b32 v137, v106, v107 offset0:64 offset1:80\n"
    "s_waitcnt vmcnt(3)\n"
    "v_mfma_f32_16x16x32_bf16 v[240:243], v[100:103], v[152:155], 0\n"
    "ds_write_b16 v142, v129 offset:128\n"
    "v_cvt_pk_f16_f32 v104, v212, v213\n"
    "v_cvt_pk_f16_f32 v105, v224, v225\n"
    "ds_write2_b32 v138, v104, v105 offset0:64 offset1:80\n"
    "v_mfma_f32_16x16x32_bf16 v[232:235], v[148:151], v[96:99], v[232:235]\n"
    "v_cvt_pk_f16_f32 v106, v214, v215\n"
    "v_cvt_pk_f16_f32 v107, v226, v227\n"
    "ds_write2_b32 v139, v106, v107 offset0:64 offset1:80\n"
    "v_mfma_f32_16x16x32_bf16 v[236:239], v[148:151], v[100:103], v[236:239]\n"
    "v_cvt_pk_f16_f32 v104, v216, v217\n"
    "v_cvt_pk_f16_f32 v105, v228, v229\n"
    "ds_write2_b32 v140, v104, v105 offset0:64 offset1:80\n"
    "v_mfma_f32_16x16x32_bf16 v[240:243], v[152:155], v[100:103], v[240:243]\n"
    "v_cvt_pk_f16_f32 v106, v218, v219\n"
    "v_cvt_pk_f16_f32 v107, v230, v231\n"
    "ds_write2_b32 v141, v106, v107 offset0:64 offset1:80\n"
    "ds_read_u16 v130, v146 offset:256\n"
    "s_cmp_lg_u32 s47, 0\n"
    "s_cbranch_scc1 Lsy\n"
    "Lsyr:\n"
    "s_waitcnt vmcnt(2)\n"
    "v_lshlrev_b32 v112, 16, v8\n"
    "v_and_b32 v113, 0xffff0000, v8\n"
    "v_cvt_pk_f16_f32 v116, v112, v113\n"
    "v_max3_f32 v248, |v112|, |v113|, 0\n"
    "v_lshlrev_b32 v114, 16, v9\n"
    "v_and_b32 v115, 0xffff0000, v9\n"
    "v_cvt_pk_f16_f32 v117, v114, v115\n"
    "v_max3_f32 v248, |v114|, |v115|, v248\n"
    "v_lshlrev_b32 v112, 16, v10\n"
    "v_and_b32 v113, 0xffff0000, v10\n"
    "v_cvt_pk_f16_f32 v118, v112, v113\n"
    "v_max3_f32 v248, |v112|, |v113|, v248\n"
    "v_lshlrev_b32 v114, 16, v11\n"
    "v_and_b32 v115, 0xffff0000, v11\n"
    "v_cvt_pk_f16_f32 v119, v114, v115\n"
    "v_max3_f32 v248, |v114|, |v115|, v248\n"
    "ds_write_b128 v42, v[116:119] offset:0\n"
    "s_waitcnt lgkmcnt(1)\n"
    "v_mul_f16 v130, 0.5, v130\n"
    "ds_write_b16 v142, v130 offset:256\n"
    "s_cmp_ge_u32 s65, 2\n"
    "s_cbranch_scc1 Ly6\n"
    "s_mul_i32 s56, s65, 48\n"
    "v_add_u32 v136, s56, v136\n"
    "v_add_u32 v137, s56, v137\n"
    "v_add_u32 v138, s56, v138\n"
    "v_add_u32 v139, s56, v139\n"
    "v_add_u32 v140, s56, v140\n"
    "v_add_u32 v141, s56, v141\n"
    "v_add_u32 v146, s56, v146\n"
    "v_add_u32 v142, s56, v142\n"
    "v_cvt_pk_f16_f32 v104, v232, v233\n"
    "v_cvt_pk_f16_f32 v105, v234, v235\n"
    "ds_write_b32 v136, v104 offset:384\n"
    "v_cvt_pk_f16_f32 v106, v236, v237\n"
    "ds_write_b32 v137, v105 offset:384\n"
    "v_cvt_pk_f16_f32 v107, v238, v239\n"
    "ds_write_b32 v138, v106 offset:384\n"
    "v_cvt_pk_f16_f32 v104, v240, v241\n"
    "ds_write_b32 v139, v107 offset:384\n"
    "v_cvt_pk_f16_f32 v105, v242, v243\n"
    "ds_write_b32 v140, v104 offset:384\n"
    "ds_write_b32 v141, v105 offset:384\n"
    "ds_read_u16 v131, v146 offset:384\n"
    "Ly6:\n"
    "v_and_b32 v40, 1, v28\n"
    "s_lshl_b32 s53, s65, 13\n"
    "v_xor_b32 v40, v183, v40\n"
    "s_mov_b32 m0, s53\n"
    "v_lshlrev_b32 v40, 4, v40\n"
    "s_waitcnt vmcnt(1)\n"
    "v_lshlrev_b32 v120, 16, v12\n"
    "v_and_b32 v121, 0xffff0000, v12\n"
    "v_cvt_pk_f16_f32 v124, v120, v121\n"
    "v_lshlrev_b32 v122, 16, v13\n"
    "v_and_b32 v123, 0xffff0000, v13\n"
    "v_cvt_pk_f16_f32 v125, v122, v123\n"
    "v_lshlrev_b32 v120, 16, v14\n"
    "v_and_b32 v121, 0xffff0000, v14\n"
    "v_cvt_pk_f16_f32 v126, v120, v121\n"
    "v_lshlrev_b32 v122, 16, v15\n"
    "v_and_b32 v123, 0xffff0000, v15\n"
    "v_cvt_pk_f16_f32 v127, v122, v123\n"
    "ds_write_b128 v42, v[124:127] offset:1024\n"
    "s_cmp_ge_u32 s65, 2\n"
    "s_cbranch_scc1 Ly6b\n"
    "s_mov_b32 exec_hi, 0\n"
    "s_waitcnt lgkmcnt(1)\n"
    "v_mul_f16 v131, 0.5, v131\n"
    "ds_write_b16 v142, v131 offset:384\n"
    "s_mov_b32 exec_hi, -1\n"
    "Ly6b:\n"
    "s_waitcnt vmcnt(0)\n"
    "v_lshlrev_b32 v112, 16, v16\n"
    "v_and_b32 v113, 0xffff0000, v16\n"
    "v_cvt_pk_f16_f32 v116, v112, v113\n"
    "v_lshlrev_b32 v114, 16, v17\n"
    "v_and_b32 v115, 0xffff0000, v17\n"
    "v_cvt_pk_f16_f32 v117, v114, v115\n"
    "v_lshlrev_b32 v112, 16, v18\n"
    "v_and_b32 v113, 0xffff0000, v18\n"
    "v_cvt_pk_f16_f32 v118, v112, v113\n"
    "v_lshlrev_b32 v114, 16, v19\n"
    "v_and_b32 v115, 0xffff0000, v19\n"
    "v_cvt_pk_f16_f32 v119, v114, v115\n"
    "ds_write_b128 v42, v[116:119] offset:2048\n"
    "s_branch Ldnn\n"
    "Lxx:\n"
    "global_load_dwordx4 v[108:111], v1, %2\n"
    "s_bfe_u32 s50, s49, 0x50003\n"
    "v_lshlrev_b32 v6, 7, v183\n"
    "s_mov_b32 m0, 44672\n"
    "v_lshlrev_b32 v32, 3, v31\n"
    "s_mov_b32 s22, 156\n"
    "s_mov_b32 s23, 0x20000\n"
    "v_and_b32 v35, 1, v28\n"
    "v_lshl_add_u32 v35, v35, 2, v30\n"
    "v_lshl_add_u32 v35, v35, 2, v29\n"
    "s_mul_i32 s52, s65, 10240\n"
    "v_add_u32 v3, 35152, v2\n"
    "s_add_u32 s52, s52, 29696\n"
    "v_mov_b32 v60, 0\n"
    "v_mov_b32 v61, 0\n"
    "s_cmp_lg_u32 s65, 0\n"
    "s_cbranch_scc1 Lnowl\n"
    "v_lshlrev_b32 v25, 1, v26\n"
    "v_cmp_eq_u32 vcc, 28, v26\n"
    "s_waitcnt lgkmcnt(0)\n"
    "buffer_load_short_d16_hi v92, v25, s[20:23], 0 offen offset:52\n"
    "v_lshrrev_b32 v24, 4, v26\n"
    "v_cndmask_b32_e64 v25, v25, 50, vcc\n"
    "v_and_b32 v23, 1, v30\n"
    "buffer_load_short_d16_hi v93, v25, s[20:23], 0 offen\n"
    "v_lshl_add_u32 v24, v23, 1, v24\n"
    "v_lshl_add_u32 v24, v31, 2, v24\n"
    "v_and_b32 v23, 8, v26\n"
    "v_lshl_add_u32 v24, v23, 1, v24\n"
    "v_lshlrev_b32 v24, 1, v24\n"
    "buffer_load_ushort v94, v24, s[20:23], 0 offen offset:104\n"
    "Lnowl:\n"
    "s_cmp_lg_u32 s65, 1\n"
    "s_cbranch_scc1 Lnt1\n"
    "s_mul_i32 s53, s50, 3328\n"
    "s_mov_b64 exec, 0x3ffffff\n"
    "s_waitcnt lgkmcnt(0)\n"
    "buffer_load_dword v6, s[16:19], s53 offen lds\n"
    "s_mov_b64 exec, -1\n"
    "Lnt1:\n"
    "s_add_u32 m0, s52, 45696\n"
    "s_nop 0\n"
    "s_waitcnt lgkmcnt(0)\n"
    "buffer_load_dwordx4 v3, s[16:19], s52 offen offset:0 lds\n"
    "s_lshl_b32 s51, s65, 6\n"
    "s_add_u32 s53, s51, 80848\n"
    "v_add_u32 v37, s53, v32\n"
    "s_movk_i32 s54, 1352\n"
    "buffer_load_dwordx4 v3, s[16:19], s52 offen offset:1024 lds\n"
    "v_add_u32 v39, 24, v29\n"
    "v_min_u32 v39, v39, v35\n"
    "v_mad_u32_u24 v112, v39, s54, v37\n"
    "v_add_u32 v39, 20, v29\n"
    "buffer_load_dwordx4 v3, s[16:19], s52 offen offset:2048 lds\n"
    "v_min_u32 v39, v39, v35\n"
    "v_add_u32 v37, 2704, v37\n"
    "v_mad_u32_u24 v113, v39, s54, v37\n"
    "s_lshl_b32 s53, s65, 5\n"
    "buffer_load_dwordx4 v3, s[16:19], s52 offen offset:3072 lds\n"
    "s_add_u32 s52, s52, 4096\n"
    "s_add_u32 m0, s52, 45696\n"
    "v_lshl_add_u32 v39, v29, 4, v27\n"
    "v_add_u32 v39, s53, v39\n"
    "v_and_b32 v42, 1, v28\n"
    "v_mul_u32_u24 v43, 2164, v42\n"
    "buffer_load_dwordx4 v3, s[16:19], s52 offen offset:0 lds\n"
    "v_lshl_add_u32 v43, v39, 1, v43\n"
    "v_add_u32 v43, 9216, v43\n"
    "v_mul_u32_u24 v44, 2521, v39\n"
    "v_lshrrev_b32 v44, 16, v44\n"
    "buffer_load_dwordx4 v3, s[16:19], s52 offen offset:1024 lds\n"
    "v_mad_u32_u24 v84, v44, 20, v43\n"
    "v_add_u32 v39, 128, v39\n"
    "v_mul_u32_u24 v44, 2521, v39\n"
    "v_lshrrev_b32 v44, 16, v44\n"
    "buffer_load_dwordx4 v3, s[16:19], s52 offen offset:2048 lds\n"
    "v_mad_u32_u24 v85, v44, 20, v43\n"
    "v_add_u32 v39, 128, v39\n"
    "v_mul_u32_u24 v44, 2521, v39\n"
    "v_lshrrev_b32 v44, 16, v44\n"
    "buffer_load_dwordx4 v3, s[16:19], s52 offen offset:3072 lds\n"
    "s_add_u32 s52, s52, 4096\n"
    "s_add_u32 m0, s52, 45696\n"
    "v_mad_u32_u24 v86, v44, 20, v43\n"
    "v_add_u32 v39, 128, v39\n"
    "v_mul_u32_u24 v44, 2521, v39\n"
    "v_lshrrev_b32 v44, 16, v44\n"
    "buffer_load_dwordx4 v3, s[16:19], s52 offen offset:0 lds\n"
    "v_mad_u32_u24 v87, v44, 20, v43\n"
    "v_add_u32 v39, 128, v39\n"
    "v_mul_u32_u24 v44, 2521, v39\n"
    "v_lshrrev_b32 v44, 16, v44\n"
    "buffer_load_dwordx4 v3, s[16:19], s52 offen offset:1024 lds\n"
    "v_mad_u32_u24 v88, v44, 20, v43\n"
    "v_add_u32 v39, 128, v39\n"
    "v_mul_u32_u24 v44, 2521, v39\n"
    "v_lshrrev_b32 v44, 16, v44\n"
    "v_mad_u32_u24 v89, v44, 20, v43\n"
    "v_cmp_gt_u32 vcc, 8, v27\n"
    "v_lshlrev_b32 v39, 4, v28\n"
    "v_add_u32 v39, 14816, v39\n"
    "v_mov_b32 v36, 44416\n"
    "v_cndmask_b32 v24, v36, v39, vcc\n"
    "v_cndmask_b32 v25, v39, v36, vcc\n"
    "s_cmp_lg_u32 s65, 1\n"
    "s_cbranch_scc1 Lnoz\n"
    "ds_write_b32 v34, v61 offset:44416\n"
    "v_min_u32 v39, 6, v183\n"
    "v_mul_u32_u24 v39, 208, v39\n"
    "ds_write_b64 v39, v[60:61] offset:13536\n"
    "v_add_u32 v39, 116000, v2\n"
    "ds_write_b32 v39, v61 offset:23932\n"
    "v_lshlrev_b32 v39, 4, v26\n"
    "v_add_u32 v39, 116000, v39\n"
    "ds_write_b32 v39, v61 offset:524\n"
    "ds_write_b16 v39, v61 offset:19242\n"
    "ds_write_b16 v39, v61 offset:43162\n"
    "Lnoz:\n"
    "s_cmp_lg_u32 s65, 0\n"
    "s_cbranch_scc1 Lnow\n"
    "v_lshlrev_b32 v39, 1, v26\n"
    "s_waitcnt vmcnt(10)\n"
    "ds_write_b16 v39, v94 offset:14816\n"
    "v_mul_u32_u24 v39, 2160, v29\n"
    "v_mul_u32_u24 v43, 72, v26\n"
    "v_and_b32 v92, 0xffff0000, v92\n"
    "v_and_b32 v93, 0xffff0000, v93\n"
    "v_add_u32 v39, v39, v43\n"
    "s_sub_i32 s53, s46, s55\n"
    "v_ldexp_f32 v92, v92, s53\n"
    "v_ldexp_f32 v93, v93, s46\n"
    "s_mov_b32 exec_lo, 0x3ffffff\n"
    "s_mov_b32 exec_hi, 0x3ffffff\n"
    "ds_write_b16_d16_hi v39, v92 offset:9272\n"
    "s_mov_b64 exec, 0xffffffff\n"
    "ds_write_b32 v34, v93 offset:44288\n"
    "s_mov_b64 exec, -1\n"
    "Lnow:\n"
    "s_mul_i32 s53, s73, 2160\n"
    "v_mul_u32_u24 v39, 72, v35\n"
    "s_add_u32 s53, s53, 9216\n"
    "v_cmp_gt_u32 s[88:89], 26, v35\n"
    "v_cmp_gt_u32 vcc, 24, v35\n"
    "v_add3_u32 v39, v39, v32, s53\n"
    "v_add_u32 v43, 144, v39\n"
    "v_cndmask_b32 v116, v36, v39, s[88:89]\n"
    "v_cndmask_b32 v117, v36, v43, vcc\n"
    "s_mul_i32 s74, s65, -12\n"
    "s_lshl_b32 s51, s73, 2\n"
    "s_sub_u32 s74, s74, s51\n"
    "s_sub_u32 s74, s74, 35152\n"
    "v_cmp_eq_u32 vcc, 3, v31\n"
    "v_mov_b32 v40, 13536\n"
    "s_add_u32 s53, s74, 32\n"
    "v_add_u32 v39, s53, v112\n"
    "v_cndmask_b32 v114, v39, v40, vcc\n"
    "v_add_u32 v39, s53, v113\n"
    "v_cndmask_b32 v115, v39, v40, vcc\n"
    "v_add_u32 v130, 80848, v32\n"
    "s_lshl_b32 s51, s65, 11\n"
    "s_add_u32 s51, s51, 32768\n"
    "v_lshl_add_u32 v33, v29, 2, v30\n"
    "v_lshlrev_b32 v33, 4, v33\n"
    "v_lshl_add_u32 v33, v42, 7, v33\n"
    "v_add_u32 v132, s51, v33\n"
    "v_add_u32 v131, 116000, v2\n"
    "s_mov_b32 exec_lo, 0xee000000\n"
    "s_mov_b32 exec_hi, 0xee000000\n"
    "v_mov_b32 v131, 0x30000\n"
    "s_mov_b64 exec, -1\n"
    "v_add_u32 v133, s51, v34\n"
    "s_mul_i32 s53, s65, 5120\n"
    "v_add_u32 v33, s53, v2\n"
    "v_add_u32 v42, 80848, v33\n"
    "s_movk_i32 s75, 4096\n"
    "s_lshl_b32 s53, s65, 3\n"
    "v_mul_u32_u24 v36, 104, v26\n"
    "v_lshl_add_u32 v36, v29, 2, v36\n"
    "s_add_u32 s53, s53, 2048\n"
    "v_add_u32 v36, s53, v36\n"
    "s_lshl_b32 s53, s65, 8\n"
    "v_add_u32 v35, s53, v34\n"
    "s_cmp_eq_u32 s65, 0\n"
    "s_cselect_b32 s53, 0, 0x28000\n"
    "v_add_u32 v37, s53, v34\n"
    "s_lshl_b32 s53, s65, 4\n"
    "s_waitcnt vmcnt(10)\n"
    "v_mad_u32_u16 v136, v108, 4, s53\n"
    "v_mad_u32_u16 v137, v108, 4, s53 op_sel:[1,0,0,0]\n"
    "v_mad_u32_u16 v138, v109, 4, s53\n"
    "v_mad_u32_u16 v139, v109, 4, s53 op_sel:[1,0,0,0]\n"
    "v_mad_u32_u16 v140, v110, 4, s53\n"
    "v_mad_u32_u16 v141, v110, 4, s53 op_sel:[1,0,0,0]\n"
    "v_lshl_add_u32 v39, v41, 1, s53\n"
    "s_add_u32 s53, s53, 116000\n"
    "v_mad_u32_u16 v146, v111, 4, v39\n"
    "v_mad_u32_u16 v142, v111, 2, s53 op_sel:[1,0,0,0]\n"
    "s_sub_i32 s51, s46, s55\n"
    "s_sub_i32 s51, s51, s55\n"
    "s_setprio 1\n"
    "s_waitcnt vmcnt(0)\n"
    "s_waitcnt lgkmcnt(0)\n"
    "s_barrier\n"
    "ds_read_b128 v[96:99], v24\n"
    "ds_read_b128 v[100:103], v25\n"
    "ds_read_b64_tr_b16 v[184:185], v112 offset:0\n"
    "ds_read_b64_tr_b16 v[186:187], v113 offset:0\n"
    "ds_read_b64_tr_b16 v[188:189], v112 offset:32\n"
    "ds_read_b64_tr_b16 v[190:191], v113 offset:32\n"
    "ds_read_b64_tr_b16 v[192:193], v112 offset:256\n"
    "ds_read_b64_tr_b16 v[194:195], v113 offset:256\n"
    "ds_read_b64_tr_b16 v[196:197], v112 offset:288\n"
    "ds_read_b64_tr_b16 v[198:199], v113 offset:288\n"
    "ds_read_b64_tr_b16 v[200:201], v112 offset:512\n"
    "ds_read_b64_tr_b16 v[202:203], v113 offset:512\n"
    "ds_read_b64_tr_b16 v[204:205], v112 offset:544\n"
    "ds_read_b64_tr_b16 v[206:207], v113 offset:544\n"
    "s_waitcnt lgkmcnt(10)\n"
    "v_mfma_f32_16x16x32_bf16 v[208:211], v[96:99], v[184:187], 0\n"
    "s_waitcnt lgkmcnt(8)\n"
    "v_mfma_f32_16x16x32_bf16 v[208:211], v[100:103], v[188:191], v[208:211]\n"
    "ds_read_b64_tr_b16 v[148:149], v112 offset:768\n"
    "ds_read_b64_tr_b16 v[150:151], v113 offset:768\n"
    "ds_read_b64_tr_b16 v[152:153], v112 offset:800\n"
    "ds_read_b64_tr_b16 v[154:155], v113 offset:800\n"
    "s_waitcnt lgkmcnt(10)\n"
    "v_mfma_f32_16x16x32_bf16 v[212:215], v[96:99], v[192:195], 0\n"
    "s_waitcnt lgkmcnt(8)\n"
    "v_mfma_f32_16x16x32_bf16 v[212:215], v[100:103], v[196:199], v[212:215]\n"
    "ds_read_b64_tr_b16 v[156:157], v112 offset:1024\n"
    "ds_read_b64_tr_b16 v[158:159], v113 offset:1024\n"
    "ds_read_b64_tr_b16 v[160:161], v112 offset:1056\n"
    "ds_read_b64_tr_b16 v[162:163], v113 offset:1056\n"
    "s_waitcnt lgkmcnt(10)\n"
    "v_mfma_f32_16x16x32_bf16 v[216:219], v[96:99], v[200:203], 0\n"
    "s_waitcnt lgkmcnt(8)\n"
    "v_mfma_f32_16x16x32_bf16 v[216:219], v[100:103], v[204:207], v[216:219]\n"
    "ds_read_b64_tr_b16 v[164:165], v112 offset:1280\n"
    "ds_read_b64_tr_b16 v[166:167], v113 offset:1280\n"
    "ds_read_b64_tr_b16 v[168:169], v112 offset:1312\n"
    "ds_read_b64_tr_b16 v[170:171], v113 offset:1312\n"
    "v_ldexp_f32 v44, v208, s51\n"
    "v_cvt_pk_bf16_f32 v44, v44, v44\n"
    "ds_write_b16 v84, v44 offset:0\n"
    "s_waitcnt lgkmcnt(11)\n"
    "v_mfma_f32_16x16x32_bf16 v[220:223], v[96:99], v[148:151], 0\n"
    "s_waitcnt lgkmcnt(9)\n"
    "v_mfma_f32_16x16x32_bf16 v[220:223], v[100:103], v[152:155], v[220:223]\n"
    "v_ldexp_f32 v45, v212, s51\n"
    "v_cvt_pk_bf16_f32 v45, v45, v45\n"
    "ds_write_b16 v85, v45 offset:256\n"
    "s_waitcnt lgkmcnt(8)\n"
    "v_mfma_f32_16x16x32_bf16 v[224:227], v[96:99], v[156:159], 0\n"
    "s_waitcnt lgkmcnt(6)\n"
    "v_mfma_f32_16x16x32_bf16 v[224:227], v[100:103], v[160:163], v[224:227]\n"
    "v_ldexp_f32 v46, v216, s51\n"
    "v_cvt_pk_bf16_f32 v46, v46, v46\n"
    "ds_write_b16 v86, v46 offset:512\n"
    "s_waitcnt lgkmcnt(5)\n"
    "v_mfma_f32_16x16x32_bf16 v[228:231], v[96:99], v[164:167], 0\n"
    "s_waitcnt lgkmcnt(3)\n"
    "v_mfma_f32_16x16x32_bf16 v[228:231], v[100:103], v[168:171], v[228:231]\n"
    "v_ldexp_f32 v47, v220, s51\n"
    "v_cvt_pk_bf16_f32 v47, v47, v47\n"
    "ds_write_b16 v87, v47 offset:768\n"
    "v_ldexp_f32 v48, v224, s51\n"
    "v_cvt_pk_bf16_f32 v48, v48, v48\n"
    "ds_write_b16 v88, v48 offset:1024\n"
    "s_nop 1\n"
    "v_ldexp_f32 v49, v228, s51\n"
    "v_cvt_pk_bf16_f32 v49, v49, v49\n"
    "ds_write_b16 v89, v49 offset:1280\n"
    "v_add_u32 v112, s74, v112\n"
    "v_add_u32 v113, s74, v113\n"
    "s_waitcnt lgkmcnt(0)\n"
    "s_barrier\n"
    "ds_read_b64_tr_b16 v[96:97], v116 offset:0\n"
    "ds_read_b64_tr_b16 v[98:99], v117 offset:0\n"
    "ds_read_b64_tr_b16 v[100:101], v116 offset:32\n"
    "ds_read_b64_tr_b16 v[102:103], v117 offset:32\n"
    "ds_read_b64_tr_b16 v[184:185], v112 offset:0\n"
    "ds_read_b64_tr_b16 v[186:187], v113 offset:0\n"
    "ds_read_b64_tr_b16 v[188:189], v114 offset:0\n"
    "ds_read_b64_tr_b16 v[190:191], v115 offset:0\n"
    "ds_read_b64_tr_b16 v[192:193], v112 offset:208\n"
    "ds_read_b64_tr_b16 v[194:195], v113 offset:208\n"
    "ds_read_b64_tr_b16 v[196:197], v114 offset:208\n"
    "ds_read_b64_tr_b16 v[198:199], v115 offset:208\n"
    "ds_read_b64_tr_b16 v[200:201], v112 offset:416\n"
    "ds_read_b64_tr_b16 v[202:203], v113 offset:416\n"
    "ds_read_b64_tr_b16 v[204:205], v114 offset:416\n"
    "ds_read_b64_tr_b16 v[206:207], v115 offset:416\n"
    "ds_read_b64_tr_b16 v[172:173], v112 offset:624\n"
    "ds_read_b64_tr_b16 v[174:175], v113 offset:624\n"
    "ds_read_b64_tr_b16 v[176:177], v114 offset:624\n"
    "ds_read_b64_tr_b16 v[178:179], v115 offset:624\n"
    "v_lshlrev_b32 v0, 4, v29\n"
    "s_mov_b32 m0, 44672\n"
    "s_mov_b32 m0, 44672\n"
    "s_cmp_lg_u32 s65, 2\n"
    "s_cbranch_scc1 Lnt2\n"
    "s_lshl_b32 s53, s50, 10\n"
    "s_mov_b64 exec, 0xff\n"
    "buffer_load_dword v6, s[12:15], s53 offen lds\n"
    "s_getpc_b64 s[68:69]\n"
    "Lpc:\n"
    "s_mov_b32 s70, Lend-Lpc\n"
    "s_mov_b32 s71, 0x20000\n"
    "s_mul_i32 s53, s50, 640\n"
    "s_mov_b64 exec, 0x1f\n"
    "buffer_load_dword v6, s[68:71], s53 offen lds\n"
    "s_mov_b64 exec, -1\n"
    "Lnt2:\n"
    "s_cmp_lg_u32 s65, 3\n"
    "s_cbranch_scc1 Lnt3\n"
    "s_mov_b32 s26, 346112\n"
    "s_mov_b32 s27, 0x20000\n"
    "s_mul_i32 s53, s50, 10880\n"
    "buffer_load_dword v6, s[24:27], s53 offen lds\n"
    "s_add_u32 s53, s53, 8192\n"
    "s_mov_b64 exec, 0x1fffff\n"
    "buffer_load_dword v6, s[24:27], s53 offen lds\n"
    "s_mov_b64 exec, -1\n"
    "Lnt3:\n"
    "s_waitcnt lgkmcnt(14)\n"
    "v_mfma_f32_16x16x32_bf16 v[208:211], v[184:187], v[96:99], 0\n"
    "v_mfma_f32_16x16x32_bf16 v[212:215], v[184:187], v[100:103], 0\n"
    "s_waitcnt lgkmcnt(12)\n"
    "v_mfma_f32_16x16x32_bf16 v[216:219], v[188:191], v[100:103], 0\n"
    "v_mfma_f32_16x16x32_bf16 v[208:211], v[96:99], v[184:187], v[208:211]\n"
    "v_mfma_f32_16x16x32_bf16 v[212:215], v[96:99], v[188:191], v[212:215]\n"
    "v_mfma_f32_16x16x32_bf16 v[216:219], v[100:103], v[188:191], v[216:219]\n"
    "ds_read_b64_tr_b16 v[184:185], v112 offset:832\n"
    "ds_read_b64_tr_b16 v[186:187], v113 offset:832\n"
    "ds_read_b64_tr_b16 v[188:189], v114 offset:832\n"
    "ds_read_b64_tr_b16 v[190:191], v115 offset:832\n"
    "s_waitcnt lgkmcnt(14)\n"
    "v_mfma_f32_16x16x32_bf16 v[220:223], v[192:195], v[96:99], 0\n"
    "v_mfma_f32_16x16x32_bf16 v[224:227], v[192:195], v[100:103], 0\n"
    "s_waitcnt lgkmcnt(12)\n"
    "v_mfma_f32_16x16x32_bf16 v[228:231], v[196:199], v[100:103], 0\n"
    "v_mfma_f32_16x16x32_bf16 v[220:223], v[96:99], v[192:195], v[220:223]\n"
    "v_mfma_f32_16x16x32_bf16 v[224:227], v[96:99], v[196:199], v[224:227]\n"
    "v_mfma_f32_16x16x32_bf16 v[228:231], v[100:103], v[196:199], v[228:231]\n"
    "ds_read_b64_tr_b16 v[192:193], v112 offset:1040\n"
    "ds_read_b64_tr_b16 v[194:195], v113 offset:1040\n"
    "ds_read_b64_tr_b16 v[196:197], v114 offset:1040\n"
    "ds_read_b64_tr_b16 v[198:199], v115 offset:1040\n"
    "s_waitcnt lgkmcnt(14)\n"
    "v_mfma_f32_16x16x32_bf16 v[232:235], v[200:203], v[96:99], 0\n"
    "v_cvt_pk_f16_f32 v104, v208, v209\n"
    "v_cvt_pk_f16_f32 v105, v220, v221\n"
    "ds_write2_b32 v136, v104, v105 offset0:0 offset1:16\n"
    "v_mfma_f32_16x16x32_bf16 v[236:239], v[200:203], v[100:103], 0\n"
    "s_waitcnt lgkmcnt(13)\n"
    "v_mfma_f32_16x16x32_bf16 v[240:243], v[204:207], v[100:103], 0\n"
    "buffer_load_dwordx4 v[8:11], v33, s[12:15], 0 offen offset:0\n"
    "v_cvt_pk_f16_f32 v106, v210, v211\n"
    "v_cvt_pk_f16_f32 v107, v222, v223\n"
    "ds_write2_b32 v137, v106, v107 offset0:0 offset1:16\n"
    "v_mfma_f32_16x16x32_bf16 v[232:235], v[96:99], v[200:203], v[232:235]\n"
    "v_mfma_f32_16x16x32_bf16 v[236:239], v[96:99], v[204:207], v[236:239]\n"
    "buffer_load_dwordx4 v[12:15], v33, s[12:15], 0 offen offset:1024\n"
    "v_cvt_pk_f16_f32 v104, v212, v213\n"
    "v_cvt_pk_f16_f32 v105, v224, v225\n"
    "ds_write2_b32 v138, v104, v105 offset0:0 offset1:16\n"
    "v_mfma_f32_16x16x32_bf16 v[240:243], v[100:103], v[204:207], v[240:243]\n"
    "s_waitcnt lgkmcnt(13)\n"
    "v_mfma_f32_16x16x32_bf16 v[244:247], v[172:175], v[96:99], 0\n"
    "buffer_load_dwordx4 v[16:19], v33, s[12:15], 0 offen offset:2048\n"
    "v_cvt_pk_f16_f32 v106, v214, v215\n"
    "v_cvt_pk_f16_f32 v107, v226, v227\n"
    "ds_write2_b32 v139, v106, v107 offset0:0 offset1:16\n"
    "v_mfma_f32_16x16x32_bf16 v[248:251], v[172:175], v[100:103], 0\n"
    "s_waitcnt lgkmcnt(12)\n"
    "v_mfma_f32_16x16x32_bf16 v[252:255], v[176:179], v[100:103], 0\n"
    "buffer_load_dwordx4 v[20:23], v33, s[12:15], 0 offen offset:3072\n"
    "v_cvt_pk_f16_f32 v104, v216, v217\n"
    "v_cvt_pk_f16_f32 v105, v228, v229\n"
    "ds_write2_b32 v140, v104, v105 offset0:0 offset1:16\n"
    "v_mfma_f32_16x16x32_bf16 v[244:247], v[96:99], v[172:175], v[244:247]\n"
    "v_mfma_f32_16x16x32_bf16 v[248:251], v[96:99], v[176:179], v[248:251]\n"
    "buffer_load_dwordx4 v[156:159], v33, s[12:15], s75 offen offset:0\n"
    "v_cvt_pk_f16_f32 v106, v218, v219\n"
    "v_cvt_pk_f16_f32 v107, v230, v231\n"
    "ds_write2_b32 v141, v106, v107 offset0:0 offset1:16\n"
    "v_mfma_f32_16x16x32_bf16 v[252:255], v[100:103], v[176:179], v[252:255]\n"
    "ds_read_b64_tr_b16 v[200:201], v112 offset:1248\n"
    "ds_read_b64_tr_b16 v[202:203], v113 offset:1248\n"
    "ds_read_b64_tr_b16 v[204:205], v114 offset:1248\n"
    "ds_read_b64_tr_b16 v[206:207], v115 offset:1248\n"
    "ds_read_u16 v90, v146 offset:0\n"
    "ds_read2st64_b32 v[148:149], v36 offset0:0 offset1:13\n"
    "v_add_u32 v36, 32, v36\n"
    "ds_read2st64_b32 v[150:151], v36 offset0:0 offset1:13\n"
    "v_add_u32 v36, 32, v36\n"
    "ds_read2st64_b32 v[152:153], v36 offset0:0 offset1:13\n"
    "v_add_u32 v36, 32, v36\n"
    "ds_read2st64_b32 v[154:155], v36 offset0:0 offset1:13\n"
    "s_waitcnt lgkmcnt(15)\n"
    "v_mfma_f32_16x16x32_bf16 v[208:211], v[184:187], v[96:99], 0\n"
    "v_cvt_pk_f16_f32 v104, v232, v233\n"
    "v_cvt_pk_f16_f32 v105, v244, v245\n"
    "ds_write2_b32 v136, v104, v105 offset0:32 offset1:48\n"
    "v_mfma_f32_16x16x32_bf16 v[212:215], v[184:187], v[100:103], 0\n"
    "v_mfma_f32_16x16x32_bf16 v[216:219], v[188:191], v[100:103], 0\n"
    "s_waitcnt lgkmcnt(5)\n"
    "v_mul_f16 v90, 0.5, v90\n"
    "ds_write_b16 v142, v90 offset:0\n"
    "s_waitcnt lgkmcnt(5)\n"
    "v_lshl_or_b32 v43, v149, 16, v148\n"
    "v_and_b32 v39, 0x80008, v43\n"
    "v_cvt_pk_f16_f32 v106, v234, v235\n"
    "v_cvt_pk_f16_f32 v107, v246, v247\n"
    "ds_write2_b32 v137, v106, v107 offset0:32 offset1:48\n"
    "v_mfma_f32_16x16x32_bf16 v[208:211], v[96:99], v[184:187], v[208:211]\n"
    "v_mfma_f32_16x16x32_bf16 v[212:215], v[96:99], v[188:191], v[212:215]\n"
    "v_lshlrev_b32 v43, 5, v43\n"
    "v_lshl_or_b32 v148, v39, 1, v43\n"
    "ds_write_b32 v35, v148 offset:40960\n"
    "s_waitcnt lgkmcnt(6)\n"
    "v_lshl_or_b32 v43, v151, 16, v150\n"
    "v_cvt_pk_f16_f32 v104, v236, v237\n"
    "v_cvt_pk_f16_f32 v105, v248, v249\n"
    "ds_write2_b32 v138, v104, v105 offset0:32 offset1:48\n"
    "v_mfma_f32_16x16x32_bf16 v[216:219], v[100:103], v[188:191], v[216:219]\n"
    "v_mfma_f32_16x16x32_bf16 v[220:223], v[192:195], v[96:99], 0\n"
    "v_and_b32 v39, 0x80008, v43\n"
    "v_lshlrev_b32 v43, 5, v43\n"
    "v_lshl_or_b32 v149, v39, 1, v43\n"
    "ds_write_b32 v35, v149 offset:41984\n"
    "v_cvt_pk_f16_f32 v106, v238, v239\n"
    "v_cvt_pk_f16_f32 v107, v250, v251\n"
    "ds_write2_b32 v139, v106, v107 offset0:32 offset1:48\n"
    "v_mfma_f32_16x16x32_bf16 v[224:227], v[192:195], v[100:103], 0\n"
    "v_mfma_f32_16x16x32_bf16 v[228:231], v[196:199], v[100:103], 0\n"
    "s_waitcnt lgkmcnt(8)\n"
    "v_lshl_or_b32 v43, v153, 16, v152\n"
    "v_and_b32 v39, 0x80008, v43\n"
    "v_lshlrev_b32 v43, 5, v43\n"
    "v_lshl_or_b32 v150, v39, 1, v43\n"
    "v_cvt_pk_f16_f32 v104, v240, v241\n"
    "v_cvt_pk_f16_f32 v105, v252, v253\n"
    "ds_write2_b32 v140, v104, v105 offset0:32 offset1:48\n"
    "v_mfma_f32_16x16x32_bf16 v[220:223], v[96:99], v[192:195], v[220:223]\n"
    "v_mfma_f32_16x16x32_bf16 v[224:227], v[96:99], v[196:199], v[224:227]\n"
    "ds_write_b32 v35, v150 offset:43008\n"
    "s_waitcnt lgkmcnt(9)\n"
    "v_lshl_or_b32 v43, v155, 16, v154\n"
    "v_and_b32 v39, 0x80008, v43\n"
    "v_lshlrev_b32 v43, 5, v43\n"
    "v_cvt_pk_f16_f32 v106, v242, v243\n"
    "v_cvt_pk_f16_f32 v107, v254, v255\n"
    "ds_write2_b32 v141, v106, v107 offset0:32 offset1:48\n"
    "v_mfma_f32_16x16x32_bf16 v[228:231], v[100:103], v[196:199], v[228:231]\n"
    "v_lshl_or_b32 v151, v39, 1, v43\n"
    "ds_write_b32 v37, v151 offset:44032\n"
    "ds_read_u16 v91, v146 offset:128\n"
    "s_cmp_lg_u32 s47, 0\n"
    "s_cbranch_scc1 Lsx\n"
    "Lsxr:\n"
    "v_mfma_f32_16x16x32_bf16 v[232:235], v[200:203], v[96:99], 0\n"
    "v_cvt_pk_f16_f32 v104, v208, v209\n"
    "v_cvt_pk_f16_f32 v105, v220, v221\n"
    "ds_write2_b32 v136, v104, v105 offset0:64 offset1:80\n"
    "v_mfma_f32_16x16x32_bf16 v[236:239], v[200:203], v[100:103], 0\n"
    "s_waitcnt lgkmcnt(1)\n"
    "v_mul_f16 v91, 0.5, v91\n"
    "ds_write_b16 v142, v91 offset:128\n"
    "ds_read_b128 v[64:67], v0 offset:44288\n"
    "ds_read_b128 v[68:71], v0 offset:44320\n"
    "v_cvt_pk_f16_f32 v106, v210, v211\n"
    "v_cvt_pk_f16_f32 v107, v222, v223\n"
    "ds_write2_b32 v137, v106, v107 offset0:64 offset1:80\n"
    "v_mfma_f32_16x16x32_bf16 v[240:243], v[204:207], v[100:103], 0\n"
    "ds_read_b128 v[72:75], v0 offset:44352\n"
    "ds_read_b128 v[76:79], v0 offset:44384\n"
    "ds_read_b128 v[120:123], v132\n"
    "s_waitcnt vmcnt(4)\n"
    "v_lshlrev_b32 v48, 16, v8\n"
    "v_cvt_pk_f16_f32 v104, v212, v213\n"
    "v_cvt_pk_f16_f32 v105, v224, v225\n"
    "ds_write2_b32 v138, v104, v105 offset0:64 offset1:80\n"
    "v_mfma_f32_16x16x32_bf16 v[232:235], v[96:99], v[200:203], v[232:235]\n"
    "v_and_b32 v49, 0xffff0000, v8\n"
    "v_cvt_pk_f16_f32 v52, v48, v49\n"
    "v_lshlrev_b32 v50, 16, v9\n"
    "v_and_b32 v51, 0xffff0000, v9\n"
    "v_cvt_pk_f16_f32 v106, v214, v215\n"
    "v_cvt_pk_f16_f32 v107, v226, v227\n"
    "ds_write2_b32 v139, v106, v107 offset0:64 offset1:80\n"
    "v_mfma_f32_16x16x32_bf16 v[236:239], v[96:99], v[204:207], v[236:239]\n"
    "v_cvt_pk_f16_f32 v53, v50, v51\n"
    "v_lshlrev_b32 v48, 16, v10\n"
    "v_and_b32 v49, 0xffff0000, v10\n"
    "v_cvt_pk_f16_f32 v54, v48, v49\n"
    "v_cvt_pk_f16_f32 v104, v216, v217\n"
    "v_cvt_pk_f16_f32 v105, v228, v229\n"
    "ds_write2_b32 v140, v104, v105 offset0:64 offset1:80\n"
    "v_mfma_f32_16x16x32_bf16 v[240:243], v[100:103], v[204:207], v[240:243]\n"
    "v_lshlrev_b32 v50, 16, v11\n"
    "v_and_b32 v51, 0xffff0000, v11\n"
    "v_cvt_pk_f16_f32 v55, v50, v51\n"
    "ds_write_b128 v42, v[52:55] offset:0\n"
    "v_cvt_pk_f16_f32 v106, v218, v219\n"
    "v_cvt_pk_f16_f32 v107, v230, v231\n"
    "ds_write2_b32 v141, v106, v107 offset0:64 offset1:80\n"
    "ds_read_u16 v92, v146 offset:256\n"
    "s_waitcnt vmcnt(3)\n"
    "v_lshlrev_b32 v56, 16, v12\n"
    "v_and_b32 v57, 0xffff0000, v12\n"
    "v_cvt_pk_f16_f32 v60, v56, v57\n"
    "v_lshlrev_b32 v58, 16, v13\n"
    "v_and_b32 v59, 0xffff0000, v13\n"
    "v_cvt_pk_f16_f32 v61, v58, v59\n"
    "v_lshlrev_b32 v56, 16, v14\n"
    "v_and_b32 v57, 0xffff0000, v14\n"
    "v_cvt_pk_f16_f32 v62, v56, v57\n"
    "v_lshlrev_b32 v58, 16, v15\n"
    "v_and_b32 v59, 0xffff0000, v15\n"
    "v_cvt_pk_f16_f32 v63, v58, v59\n"
    "ds_write_b128 v42, v[60:63] offset:1024\n"
    "s_waitcnt lgkmcnt(1)\n"
    "v_mul_f16 v92, 0.5, v92\n"
    "ds_write_b16 v142, v92 offset:256\n"
    "s_cmp_ge_u32 s65, 2\n"
    "s_cbranch_scc1 Lx6\n"
    "s_mul_i32 s56, s65, 48\n"
    "v_add_u32 v136, s56, v136\n"
    "v_add_u32 v137, s56, v137\n"
    "v_add_u32 v138, s56, v138\n"
    "v_add_u32 v139, s56, v139\n"
    "v_add_u32 v140, s56, v140\n"
    "v_add_u32 v141, s56, v141\n"
    "v_add_u32 v146, s56, v146\n"
    "v_add_u32 v142, s56, v142\n"
    "v_cvt_pk_f16_f32 v104, v232, v233\n"
    "v_cvt_pk_f16_f32 v105, v234, v235\n"
    "ds_write_b32 v136, v104 offset:384\n"
    "v_cvt_pk_f16_f32 v106, v236, v237\n"
    "ds_write_b32 v137, v105 offset:384\n"
    "v_cvt_pk_f16_f32 v107, v238, v239\n"
    "ds_write_b32 v138, v106 offset:384\n"
    "v_cvt_pk_f16_f32 v104, v240, v241\n"
    "ds_write_b32 v139, v107 offset:384\n"
    "v_cvt_pk_f16_f32 v105, v242, v243\n"
    "ds_write_b32 v140, v104 offset:384\n"
    "ds_write_b32 v141, v105 offset:384\n"
    "ds_read_u16 v93, v146 offset:384\n"
    "Lx6:\n"
    "s_waitcnt vmcnt(2)\n"
    "v_lshlrev_b32 v48, 16, v16\n"
    "v_and_b32 v49, 0xffff0000, v16\n"
    "v_cvt_pk_f16_f32 v52, v48, v49\n"
    "v_lshlrev_b32 v50, 16, v17\n"
    "v_and_b32 v51, 0xffff0000, v17\n"
    "v_cvt_pk_f16_f32 v53, v50, v51\n"
    "v_lshlrev_b32 v48, 16, v18\n"
    "v_and_b32 v49, 0xffff0000, v18\n"
    "v_cvt_pk_f16_f32 v54, v48, v49\n"
    "v_lshlrev_b32 v50, 16, v19\n"
    "v_and_b32 v51, 0xffff0000, v19\n"
    "v_cvt_pk_f16_f32 v55, v50, v51\n"
    "ds_write_b128 v42, v[52:55] offset:2048\n"
    "s_cmp_ge_u32 s65, 2\n"
    "s_cbranch_scc1 Lx6b\n"
    "s_mov_b32 exec_hi, 0\n"
    "s_waitcnt lgkmcnt(1)\n"
    "v_mul_f16 v93, 0.5, v93\n"
    "ds_write_b16 v142, v93 offset:384\n"
    "s_mov_b32 exec_hi, -1\n"
    "Lx6b:\n"
    "s_waitcnt vmcnt(1)\n"
    "v_lshlrev_b32 v56, 16, v20\n"
    "v_and_b32 v57, 0xffff0000, v20\n"
    "v_cvt_pk_f16_f32 v60, v56, v57\n"
    "v_lshlrev_b32 v58, 16, v21\n"
    "v_and_b32 v59, 0xffff0000, v21\n"
    "v_cvt_pk_f16_f32 v61, v58, v59\n"
    "v_lshlrev_b32 v56, 16, v22\n"
    "v_and_b32 v57, 0xffff0000, v22\n"
    "v_cvt_pk_f16_f32 v62, v56, v57\n"
    "v_lshlrev_b32 v58, 16, v23\n"
    "v_and_b32 v59, 0xffff0000, v23\n"
    "v_cvt_pk_f16_f32 v63, v58, v59\n"
    "ds_write_b128 v42, v[60:63] offset:3072\n"
    "v_add_u32_sdwa v101, v130, v122 dst_sel:DWORD dst_unused:UNUSED_PAD src0_sel:DWORD src1_sel:WORD_1\n"
    "v_add_u32_sdwa v100, v130, v122 dst_sel:DWORD dst_unused:UNUSED_PAD src0_sel:DWORD src1_sel:WORD_0\n"
    "v_add_u32_sdwa v96, v130, v120 dst_sel:DWORD dst_unused:UNUSED_PAD src0_sel:DWORD src1_sel:WORD_0\n"
    "v_add_u32_sdwa v97, v130, v120 dst_sel:DWORD dst_unused:UNUSED_PAD src0_sel:DWORD src1_sel:WORD_1\n"
    "v_add_u32_sdwa v98, v130, v121 dst_sel:DWORD dst_unused:UNUSED_PAD src0_sel:DWORD src1_sel:WORD_0\n"
    "v_add_u32_sdwa v99, v130, v121 dst_sel:DWORD dst_unused:UNUSED_PAD src0_sel:DWORD src1_sel:WORD_1\n"
    "v_add_u32_sdwa v102, v130, v123 dst_sel:DWORD dst_unused:UNUSED_PAD src0_sel:DWORD src1_sel:WORD_0\n"
    "v_add_u32_sdwa v103, v130, v123 dst_sel:DWORD dst_unused:UNUSED_PAD src0_sel:DWORD src1_sel:WORD_1\n"
    "s_waitcnt vmcnt(0)\n"
    "v_lshlrev_b32 v48, 16, v156\n"
    "v_and_b32 v49, 0xffff0000, v156\n"
    "v_cvt_pk_f16_f32 v52, v48, v49\n"
    "v_lshlrev_b32 v50, 16, v157\n"
    "v_and_b32 v51, 0xffff0000, v157\n"
    "v_cvt_pk_f16_f32 v53, v50, v51\n"
    "v_lshlrev_b32 v48, 16, v158\n"
    "v_and_b32 v49, 0xffff0000, v158\n"
    "v_cvt_pk_f16_f32 v54, v48, v49\n"
    "v_lshlrev_b32 v50, 16, v159\n"
    "v_and_b32 v51, 0xffff0000, v159\n"
    "v_cvt_pk_f16_f32 v55, v50, v51\n"
    "ds_write_b128 v42, v[52:55] offset:4096\n"
    "s_waitcnt lgkmcnt(0)\n"
    "s_barrier\n"
    "s_setprio 1\n"
    "ds_read_b64_tr_b16 v[90:91], v101\n"
    "ds_read_b64_tr_b16 v[88:89], v100\n"
    "ds_read_b64_tr_b16 v[80:81], v96\n"
    "ds_read_b64_tr_b16 v[82:83], v97\n"
    "ds_read_b128 v[168:171], v131 offset:24960\n"
    "ds_read_b128 v[104:107], v131 offset:1040\n"
    "ds_read_b128 v[172:175], v131 offset:26000\n"
    "ds_read_b128 v[108:111], v131 offset:2080\n"
    "ds_read_b128 v[176:179], v131 offset:40560\n"
    "ds_read_b64_tr_b16 v[84:85], v98\n"
    "ds_read_b64_tr_b16 v[86:87], v99\n"
    "ds_read_b64_tr_b16 v[92:93], v102\n"
    "ds_read_b64_tr_b16 v[94:95], v103\n"
    "s_waitcnt lgkmcnt(12)\n"
    "v_pk_mul_f16 v96, v91, v91\n"
    "s_waitcnt lgkmcnt(10)\n"
    "v_pk_mul_f16 v97, v88, v80 op_sel_hi:[1,0]\n"
    "v_pk_mul_f16 v98, v88, v80 op_sel:[0,1]\n"
    "v_pk_mul_f16 v99, v88, v81 op_sel_hi:[1,0]\n"
    "v_pk_mul_f16 v100, v88, v81 op_sel:[0,1]\n"
    "s_waitcnt lgkmcnt(9)\n"
    "v_pk_mul_f16 v101, v88, v82 op_sel_hi:[1,0]\n"
    "s_mov_b32 s58, 4\n"
    "s_movk_i32 s59, 0x207\n"
    "s_mov_b64 s[60:61], 0\n"
    "s_waitcnt lgkmcnt(8)\n"
    "v_mfma_f32_32x32x16_f16 v[32:47], v[168:171], v[96:99], 0\n"
    "v_pk_mul_f16 v102, v88, v82 op_sel:[0,1]\n"
    "v_pk_mul_f16 v103, v89, v80 op_sel_hi:[1,0]\n"
    "ds_read_b128 v[112:115], v131 offset:3120\n"
    "ds_read_b128 v[116:119], v131 offset:4160\n"
    "ds_read_b128 v[136:139], v131 offset:16640\n"
    "ds_read_b128 v[180:183], v131 offset:27040\n"
    "s_waitcnt lgkmcnt(9)\n"
    "v_mfma_f32_32x32x16_f16 v[48:63], v[104:107], v[96:99], v[64:79]\n"
    "v_pk_mul_f16 v96, v89, v80 op_sel:[0,1]\n"
    "v_pk_mul_f16 v97, v89, v81 op_sel_hi:[1,0]\n"
    "ds_read_b128 v[120:123], v132 offset:256\n"
    "v_mov_b32 v4, 44544\n"
    "ds_read_b128 v[184:187], v131 offset:28080\n"
    "ds_read_b128 v[188:191], v131 offset:41600\n"
    "v_mfma_f32_32x32x16_f16 v[32:47], v[172:175], v[100:103], v[32:47]\n"
    "v_pk_mul_f16 v98, v89, v81 op_sel:[0,1]\n"
    "v_pk_mul_f16 v99, v89, v82 op_sel_hi:[1,0]\n"
    "v_mov_b32 v5, 1\n"
    "ds_read_b128 v[140:143], v131 offset:17680\n"
    "v_mfma_f32_32x32x16_f16 v[48:63], v[108:111], v[100:103], v[48:63]\n"
    "v_pk_mul_f16 v100, v89, v82 op_sel:[0,1]\n"
    "v_pk_mul_f16 v101, v90, v80 op_sel_hi:[1,0]\n"
    "ds_read_b128 v[192:195], v131 offset:29120\n"
    "ds_read_b128 v[196:199], v131 offset:30160\n"
    "s_waitcnt lgkmcnt(14)\n"
    "v_mfma_f32_32x32x16_f16 v[32:47], v[176:179], v[96:99], v[32:47]\n"
    "v_pk_mul_f16 v102, v90, v80 op_sel:[0,1]\n"
    "v_pk_mul_f16 v103, v90, v81 op_sel_hi:[1,0]\n"
    "ds_read_b128 v[104:107], v131 offset:5200\n"
    "ds_read_b128 v[200:203], v131 offset:42640\n"
    "s_waitcnt lgkmcnt(9)\n"
    "v_mfma_f32_32x32x16_f16 v[48:63], v[136:139], v[96:99], v[48:63]\n"
    "v_pk_mul_f16 v96, v90, v81 op_sel:[0,1]\n"
    "v_pk_mul_f16 v97, v90, v82 op_sel_hi:[1,0]\n"
    "ds_read_b128 v[144:147], v131 offset:18720\n"
    "ds_read_b128 v[204:207], v131 offset:31200\n"
    "s_waitcnt lgkmcnt(10)\n"
    "v_mfma_f32_32x32x16_f16 v[32:47], v[180:183], v[100:103], v[32:47]\n"
    "v_pk_mul_f16 v98, v90, v82 op_sel:[0,1]\n"
    "v_pk_mul_f16 v99, v80, v81 op_sel_hi:[1,0]\n"
    "ds_read_b128 v[208:211], v131 offset:32240\n"
    "ds_read_b128 v[212:215], v131 offset:43680\n"
    "v_mfma_f32_32x32x16_f16 v[48:63], v[112:115], v[100:103], v[48:63]\n"
    "v_pk_mul_f16 v100, v80, v81 op_sel:[0,1]\n"
    "v_pk_mul_f16 v101, v80, v82 op_sel_hi:[1,0]\n"
    "ds_read_b128 v[108:111], v131 offset:6240\n"
    "ds_read_b128 v[148:151], v131 offset:19760\n"
    "s_waitcnt lgkmcnt(12)\n"
    "v_mfma_f32_32x32x16_f16 v[32:47], v[184:187], v[96:99], v[32:47]\n"
    "v_pk_mul_f16 v102, v80, v82 op_sel:[0,1]\n"
    "v_pk_mul_f16 v103, v81, v82 op_sel_hi:[1,0]\n"
    "ds_read_b128 v[216:219], v131 offset:33280\n"
    "ds_read_b128 v[220:223], v131 offset:34320\n"
    "v_mfma_f32_32x32x16_f16 v[48:63], v[116:119], v[96:99], v[48:63]\n"
    "v_pk_mul_f16 v96, v81, v82 op_sel:[0,1]\n"
    "v_pk_mul_f16 v97, v80, v83 op_sel_hi:[1,0]\n"
    "ds_read_b128 v[112:115], v131 offset:7280\n"
    "ds_read_b128 v[224:227], v131 offset:44720\n"
    "s_waitcnt lgkmcnt(15)\n"
    "v_mfma_f32_32x32x16_f16 v[32:47], v[188:191], v[100:103], v[32:47]\n"
    "v_pk_mul_f16 v98, v80, v83 op_sel:[0,1]\n"
    "v_pk_mul_f16 v99, v81, v83 op_sel_hi:[1,0]\n"
    "ds_read_b128 v[152:155], v131 offset:20800\n"
    "ds_read_b128 v[228:231], v131 offset:35360\n"
    "s_waitcnt lgkmcnt(15)\n"
    "v_mfma_f32_32x32x16_f16 v[48:63], v[140:143], v[100:103], v[48:63]\n"
    "v_pk_mul_f16 v100, v81, v83 op_sel:[0,1]\n"
    "v_pk_mul_f16 v101, v82, v83 op_sel_hi:[1,0]\n"
    "ds_read_b128 v[232:235], v131 offset:36400\n"
    "ds_read_b128 v[164:167], v131 offset:23920\n"
    "v_mfma_f32_32x32x16_f16 v[32:47], v[192:195], v[96:99], v[32:47]\n"
    "v_pk_mul_f16 v102, v82, v83 op_sel:[0,1]\n"
    "v_pk_mul_f16 v103, v80, v80 op_sel_hi:[1,0]\n"
    "ds_read_b128 v[236:239], v131 offset:45760\n"
    "s_waitcnt lgkmcnt(10)\n"
    "v_mfma_f32_32x32x16_f16 v[48:63], v[104:107], v[96:99], v[48:63]\n"
    "v_pk_mul_f16 v96, v81, v81 op_sel_hi:[1,0]\n"
    "v_pk_mul_f16 v97, v82, v82 op_sel_hi:[1,0]\n"
    "ds_read_b128 v[156:159], v131 offset:21840\n"
    "ds_read_b128 v[240:243], v131 offset:37440\n"
    "v_mfma_f32_32x32x16_f16 v[32:47], v[196:199], v[100:103], v[32:47]\n"
    "v_pk_mul_f16 v98, v83, v83 op_sel_hi:[1,0]\n"
    "v_pk_mul_f16 v99, v84, v83 op_sel_hi:[1,0]\n"
    "ds_read_b128 v[116:119], v131 offset:0\n"
    "ds_read_b128 v[244:247], v131 offset:38480\n"
    "v_mfma_f32_32x32x16_f16 v[48:63], v[108:111], v[100:103], v[48:63]\n"
    "v_pk_mul_f16 v100, v84, v83 op_sel:[0,1]\n"
    "v_pk_mul_f16 v101, v85, v83 op_sel_hi:[1,0]\n"
    "ds_read_b128 v[248:251], v131 offset:46800\n"
    "ds_read_b128 v[160:163], v131 offset:22880\n"
    "v_mfma_f32_32x32x16_f16 v[32:47], v[200:203], v[96:99], v[32:47]\n"
    "v_pk_mul_f16 v102, v85, v83 op_sel:[0,1]\n"
    "v_pk_mul_f16 v103, v86, v83 op_sel_hi:[1,0]\n"
    "ds_read_b128 v[252:255], v131 offset:39520\n"
    "v_mfma_f32_32x32x16_f16 v[48:63], v[144:147], v[96:99], v[48:63]\n"
    "v_pk_mul_f16 v96, v86, v83 op_sel:[0,1]\n"
    "v_pk_mul_f16 v97, v84, v80 op_sel_hi:[1,0]\n"
    "ds_read_b128 v[104:107], v131 offset:8320\n"
    "ds_read_b128 v[108:111], v131 offset:9360\n"
    "v_mfma_f32_32x32x16_f16 v[32:47], v[204:207], v[100:103], v[32:47]\n"
    "v_pk_mul_f16 v98, v84, v80 op_sel:[0,1]\n"
    "v_pk_mul_f16 v99, v85, v80 op_sel_hi:[1,0]\n"
    "v_add_u32_sdwa v128, v130, v122 dst_sel:DWORD dst_unused:UNUSED_PAD src0_sel:DWORD src1_sel:WORD_0\n"
    "s_waitcnt lgkmcnt(6)\n"
    "v_mfma_f32_32x32x16_f16 v[48:63], v[112:115], v[100:103], v[48:63]\n"
    "v_pk_mul_f16 v100, v85, v80 op_sel:[0,1]\n"
    "v_pk_mul_f16 v101, v86, v80 op_sel_hi:[1,0]\n"
    "ds_read_b128 v[112:115], v131 offset:10400\n"
    "v_add_u32_sdwa v126, v130, v123 dst_sel:DWORD dst_unused:UNUSED_PAD src0_sel:DWORD src1_sel:WORD_0\n"
    "v_mfma_f32_32x32x16_f16 v[48:63], v[116:119], v[80:83], v[48:63]\n"
    "v_lshl_add_u32 v6, v29, 5, v26\n"
    "v_lshrrev_b32 v1, 3, v6\n"
    "v_lshlrev_b32 v1, 8, v1\n"
    "v_and_b32 v2, 1, v30\n"
    "v_lshl_add_u32 v1, v2, 6, v1\n"
    "s_waitcnt lgkmcnt(3)\n"
    "s_mov_b64 exec, 1\n"
    "ds_add_u32 v4, v5 offset:20\n"
    "s_mov_b64 exec, -1\n"
    "v_mfma_f32_32x32x16_f16 v[32:47], v[208:211], v[96:99], v[32:47]\n"
    "v_pk_mul_f16 v102, v86, v80 op_sel:[0,1]\n"
    "v_pk_mul_f16 v103, v84, v81 op_sel_hi:[1,0]\n"
    "v_bfe_u32 v2, v6, 1, 1\n"
    "v_lshl_add_u32 v1, v2, 7, v1\n"
    "s_waitcnt lgkmcnt(2)\n"
    "v_mfma_f32_32x32x16_f16 v[48:63], v[104:107], v[96:99], v[48:63]\n"
    "v_pk_mul_f16 v96, v84, v81 op_sel:[0,1]\n"
    "v_pk_mul_f16 v97, v85, v81 op_sel_hi:[1,0]\n"
    "v_and_b32 v2, 1, v31\n"
    "v_lshl_add_u32 v1, v2, 5, v1\n"
    "s_lshl_b32 s51, s65, 11\n"
    "v_mfma_f32_32x32x16_f16 v[32:47], v[212:215], v[100:103], v[32:47]\n"
    "v_pk_mul_f16 v98, v85, v81 op_sel:[0,1]\n"
    "v_pk_mul_f16 v99, v86, v81 op_sel_hi:[1,0]\n"
    "ds_read_b128 v[116:119], v131 offset:11440\n"
    "v_add_u32 v77, s51, v1\n"
    "v_mfma_f32_32x32x16_f16 v[48:63], v[148:151], v[100:103], v[48:63]\n"
    "v_pk_mul_f16 v100, v86, v81 op_sel:[0,1]\n"
    "v_pk_mul_f16 v101, v84, v82 op_sel_hi:[1,0]\n"
    "v_lshrrev_b32 v1, 2, v6\n"
    "s_lshl_b32 s52, s65, 4\n"
    "v_add_u32 v1, s52, v1\n"
    "v_mfma_f32_32x32x16_f16 v[32:47], v[216:219], v[96:99], v[32:47]\n"
    "v_pk_mul_f16 v102, v84, v82 op_sel:[0,1]\n"
    "v_pk_mul_f16 v103, v85, v82 op_sel_hi:[1,0]\n"
    "v_mul_u32_u24 v2, 7168, v31\n"
    "v_lshrrev_b32 v3, 5, v1\n"
    "v_mfma_f32_32x32x16_f16 v[48:63], v[108:111], v[96:99], v[48:63]\n"
    "v_pk_mul_f16 v96, v85, v82 op_sel:[0,1]\n"
    "v_pk_mul_f16 v97, v86, v82 op_sel_hi:[1,0]\n"
    "ds_read_b128 v[104:107], v131 offset:12480\n"
    "v_lshl_add_u32 v2, v3, 8, v2\n"
    "v_mfma_f32_32x32x16_f16 v[32:47], v[220:223], v[100:103], v[32:47]\n"
    "v_pk_mul_f16 v98, v86, v82 op_sel:[0,1]\n"
    "v_pk_mul_f16 v99, v84, v85 op_sel_hi:[1,0]\n"
    "v_and_b32 v3, 31, v1\n"
    "s_waitcnt lgkmcnt(1)\n"
    "v_mfma_f32_32x32x16_f16 v[48:63], v[112:115], v[100:103], v[48:63]\n"
    "v_pk_mul_f16 v100, v84, v85 op_sel:[0,1]\n"
    "v_pk_mul_f16 v101, v84, v86 op_sel_hi:[1,0]\n"
    "ds_read_b64_tr_b16 v[88:89], v128\n"
    "v_mfma_f32_32x32x16_f16 v[32:47], v[224:227], v[96:99], v[32:47]\n"
    "v_pk_mul_f16 v102, v84, v86 op_sel:[0,1]\n"
    "v_pk_mul_f16 v103, v85, v86 op_sel_hi:[1,0]\n"
    "ds_read_b128 v[108:111], v131 offset:13520\n"
    "v_add_u32_sdwa v129, v130, v122 dst_sel:DWORD dst_unused:UNUSED_PAD src0_sel:DWORD src1_sel:WORD_1\n"
    "v_mfma_f32_32x32x16_f16 v[48:63], v[152:155], v[96:99], v[48:63]\n"
    "v_pk_mul_f16 v96, v85, v86 op_sel:[0,1]\n"
    "v_pk_mul_f16 v97, v84, v84 op_sel_hi:[1,0]\n"
    "v_add_u32_sdwa v128, v130, v120 dst_sel:DWORD dst_unused:UNUSED_PAD src0_sel:DWORD src1_sel:WORD_0\n"
    "v_lshl_add_u32 v2, v3, 2, v2\n"
    "v_mfma_f32_32x32x16_f16 v[32:47], v[228:231], v[100:103], v[32:47]\n"
    "v_pk_mul_f16 v98, v85, v85 op_sel_hi:[1,0]\n"
    "v_pk_mul_f16 v99, v86, v86 op_sel_hi:[1,0]\n"
    "ds_read_b64_tr_b16 v[90:91], v129\n"
    "v_mfma_f32_32x32x16_f16 v[48:63], v[116:119], v[100:103], v[48:63]\n"
    "v_pk_mul_f16 v100, v87, v87\n"
    "v_pk_mul_f16 v101, v84, v92 op_sel_hi:[1,0]\n"
    "s_lshl_b32 s52, s49, 7\n"
    "v_lshl_add_u32 v78, v1, 1, s52\n"
    "v_mfma_f32_32x32x16_f16 v[32:47], v[232:235], v[96:99], v[32:47]\n"
    "v_pk_mul_f16 v102, v84, v92 op_sel:[0,1]\n"
    "v_pk_mul_f16 v103, v84, v93 op_sel_hi:[1,0]\n"
    "v_add_u32 v79, 45696, v2\n"
    "s_waitcnt lgkmcnt(1)\n"
    "v_mfma_f32_32x32x16_f16 v[48:63], v[104:107], v[96:99], v[48:63]\n"
    "v_pk_mul_f16 v96, v84, v93 op_sel:[0,1]\n"
    "v_pk_mul_f16 v97, v84, v94 op_sel_hi:[1,0]\n"
    "ds_read_b64_tr_b16 v[80:81], v128\n"
    "v_mfma_f32_32x32x16_f16 v[48:63], v[164:167], v[84:87], v[48:63]\n"
    "ds_read_b128 v[112:115], v131 offset:14560\n"
    "ds_read_b128 v[116:119], v131 offset:15600\n"
    "v_add_u32_sdwa v125, v130, v121 dst_sel:DWORD dst_unused:UNUSED_PAD src0_sel:DWORD src1_sel:WORD_1\n"
    "v_mfma_f32_32x32x16_f16 v[32:47], v[236:239], v[100:103], v[32:47]\n"
    "v_pk_mul_f16 v98, v84, v94 op_sel:[0,1]\n"
    "v_pk_mul_f16 v99, v85, v92 op_sel_hi:[1,0]\n"
    "ds_read_b128 v[104:107], v131 offset:1040\n"
    "v_add_u32_sdwa v129, v130, v120 dst_sel:DWORD dst_unused:UNUSED_PAD src0_sel:DWORD src1_sel:WORD_1\n"
    "v_mfma_f32_32x32x16_f16 v[48:63], v[156:159], v[100:103], v[48:63]\n"
    "v_pk_mul_f16 v100, v85, v92 op_sel:[0,1]\n"
    "v_pk_mul_f16 v101, v85, v93 op_sel_hi:[1,0]\n"
    "ds_read_b64_tr_b16 v[82:83], v129\n"
    "v_mfma_f32_32x32x16_f16 v[32:47], v[240:243], v[96:99], v[32:47]\n"
    "v_pk_mul_f16 v102, v85, v93 op_sel:[0,1]\n"
    "v_pk_mul_f16 v103, v85, v94 op_sel_hi:[1,0]\n"
    "v_add_u32_sdwa v124, v130, v121 dst_sel:DWORD dst_unused:UNUSED_PAD src0_sel:DWORD src1_sel:WORD_0\n"
    "v_add_u32_sdwa v127, v130, v123 dst_sel:DWORD dst_unused:UNUSED_PAD src0_sel:DWORD src1_sel:WORD_1\n"
    "v_mfma_f32_32x32x16_f16 v[48:63], v[108:111], v[96:99], v[48:63]\n"
    "v_pk_mul_f16 v96, v85, v94 op_sel:[0,1]\n"
    "v_pk_mul_f16 v97, v86, v92 op_sel_hi:[1,0]\n"
    "ds_read_b128 v[108:111], v131 offset:2080\n"
    "v_mfma_f32_32x32x16_f16 v[32:47], v[244:247], v[100:103], v[32:47]\n"
    "v_pk_mul_f16 v98, v86, v92 op_sel:[0,1]\n"
    "v_pk_mul_f16 v99, v86, v93 op_sel_hi:[1,0]\n"
    "ds_read_b64_tr_b16 v[84:85], v124\n"
    "s_waitcnt lgkmcnt(4)\n"
    "v_mfma_f32_32x32x16_f16 v[48:63], v[112:115], v[100:103], v[48:63]\n"
    "v_pk_mul_f16 v100, v86, v93 op_sel:[0,1]\n"
    "v_pk_mul_f16 v101, v86, v94 op_sel_hi:[1,0]\n"
    "ds_read_b128 v[120:123], v132 offset:512\n"
    "v_mfma_f32_32x32x16_f16 v[32:47], v[248:251], v[96:99], v[32:47]\n"
    "v_pk_mul_f16 v102, v86, v94 op_sel:[0,1]\n"
    "v_pk_mul_f16 v103, v95, v95\n"
    "ds_read_b64_tr_b16 v[86:87], v125\n"
    "v_mfma_f32_32x32x16_f16 v[48:63], v[160:163], v[96:99], v[48:63]\n"
    "v_pk_mul_f16 v96, v91, v91\n"
    "v_pk_mul_f16 v97, v88, v80 op_sel_hi:[1,0]\n"
    "ds_read_b128 v[112:115], v131 offset:3120\n"
    "v_mfma_f32_32x32x16_f16 v[32:47], v[252:255], v[100:103], v[32:47]\n"
    "v_pk_mul_f16 v98, v88, v80 op_sel:[0,1]\n"
    "v_pk_mul_f16 v99, v88, v81 op_sel_hi:[1,0]\n"
    "ds_read_b64_tr_b16 v[92:93], v126\n"
    "ds_read_b64_tr_b16 v[94:95], v127\n"
    "v_mfma_f32_32x32x16_f16 v[48:63], v[116:119], v[100:103], v[48:63]\n"
    "v_pk_mul_f16 v100, v88, v81 op_sel:[0,1]\n"
    "s_waitcnt lgkmcnt(7)\n"
    "v_pk_mul_f16 v101, v88, v82 op_sel_hi:[1,0]\n"
    "ds_read_b128 v[116:119], v131 offset:4160\n"
    "s_branch Lcin_b\n"
    "Lcin:\n"
    "v_mfma_f32_32x32x16_f16 v[32:47], v[168:171], v[96:99], 0\n"
    "v_pk_mul_f16 v102, v88, v82 op_sel:[0,1]\n"
    "v_pk_mul_f16 v103, v89, v80 op_sel_hi:[1,0]\n"
    "s_waitcnt lgkmcnt(5)\n"
    "v_mfma_f32_32x32x16_f16 v[48:63], v[104:107], v[96:99], v[64:79]\n"
    "v_pk_mul_f16 v96, v89, v80 op_sel:[0,1]\n"
    "v_pk_mul_f16 v97, v89, v81 op_sel_hi:[1,0]\n"
    "ds_read_b128 v[104:107], v131 offset:5200\n"
    "v_mfma_f32_32x32x16_f16 v[32:47], v[172:175], v[100:103], v[32:47]\n"
    "v_pk_mul_f16 v98, v89, v81 op_sel:[0,1]\n"
    "v_pk_mul_f16 v99, v89, v82 op_sel_hi:[1,0]\n"
    "v_mfma_f32_32x32x16_f16 v[48:63], v[108:111], v[100:103], v[48:63]\n"
    "v_pk_mul_f16 v100, v89, v82 op_sel:[0,1]\n"
    "v_pk_mul_f16 v101, v90, v80 op_sel_hi:[1,0]\n"
    "v_mul_f32 v135, v0, v16\n"
    "v_mfma_f32_32x32x16_f16 v[32:47], v[176:179], v[96:99], v[32:47]\n"
    "v_pk_mul_f16 v102, v90, v80 op_sel:[0,1]\n"
    "v_pk_mul_f16 v103, v90, v81 op_sel_hi:[1,0]\n"
    "v_fmac_f32 v135, v1, v17\n"
    "v_mfma_f32_32x32x16_f16 v[48:63], v[136:139], v[96:99], v[48:63]\n"
    "v_pk_mul_f16 v96, v90, v81 op_sel:[0,1]\n"
    "v_pk_mul_f16 v97, v90, v82 op_sel_hi:[1,0]\n"
    "v_fmac_f32 v135, v2, v18\n"
    "v_mfma_f32_32x32x16_f16 v[32:47], v[180:183], v[100:103], v[32:47]\n"
    "v_pk_mul_f16 v98, v90, v82 op_sel:[0,1]\n"
    "v_pk_mul_f16 v99, v80, v81 op_sel_hi:[1,0]\n"
    "v_fmac_f32 v135, v3, v19\n"
    "s_waitcnt lgkmcnt(1)\n"
    "v_mfma_f32_32x32x16_f16 v[48:63], v[112:115], v[100:103], v[48:63]\n"
    "v_pk_mul_f16 v100, v80, v81 op_sel:[0,1]\n"
    "v_pk_mul_f16 v101, v80, v82 op_sel_hi:[1,0]\n"
    "v_fmac_f32 v135, v4, v20\n"
    "v_mfma_f32_32x32x16_f16 v[32:47], v[184:187], v[96:99], v[32:47]\n"
    "v_pk_mul_f16 v102, v80, v82 op_sel:[0,1]\n"
    "v_pk_mul_f16 v103, v81, v82 op_sel_hi:[1,0]\n"
    "v_add_u32_sdwa v127, v130, v123 dst_sel:DWORD dst_unused:UNUSED_PAD src0_sel:DWORD src1_sel:WORD_1\n"
    "v_fmac_f32 v135, v5, v21\n"
    "v_mfma_f32_32x32x16_f16 v[48:63], v[116:119], v[96:99], v[48:63]\n"
    "v_pk_mul_f16 v96, v81, v82 op_sel:[0,1]\n"
    "v_pk_mul_f16 v97, v80, v83 op_sel_hi:[1,0]\n"
    "ds_read_b128 v[108:111], v131 offset:6240\n"
    "v_mfma_f32_32x32x16_f16 v[32:47], v[188:191], v[100:103], v[32:47]\n"
    "v_pk_mul_f16 v98, v80, v83 op_sel:[0,1]\n"
    "v_pk_mul_f16 v99, v81, v83 op_sel_hi:[1,0]\n"
    "ds_read_b128 v[112:115], v131 offset:7280\n"
    "v_mfma_f32_32x32x16_f16 v[48:63], v[140:143], v[100:103], v[48:63]\n"
    "v_pk_mul_f16 v100, v81, v83 op_sel:[0,1]\n"
    "v_pk_mul_f16 v101, v82, v83 op_sel_hi:[1,0]\n"
    "ds_read_b128 v[116:119], v131 offset:0\n"
    "v_mfma_f32_32x32x16_f16 v[32:47], v[192:195], v[96:99], v[32:47]\n"
    "v_pk_mul_f16 v102, v82, v83 op_sel:[0,1]\n"
    "v_pk_mul_f16 v103, v80, v80 op_sel_hi:[1,0]\n"
    "v_fmac_f32 v135, v6, v22\n"
    "s_waitcnt lgkmcnt(2)\n"
    "v_mfma_f32_32x32x16_f16 v[48:63], v[104:107], v[96:99], v[48:63]\n"
    "v_pk_mul_f16 v96, v81, v81 op_sel_hi:[1,0]\n"
    "v_pk_mul_f16 v97, v82, v82 op_sel_hi:[1,0]\n"
    "v_add_u32_sdwa v125, v130, v121 dst_sel:DWORD dst_unused:UNUSED_PAD src0_sel:DWORD src1_sel:WORD_1\n"
    "v_mfma_f32_32x32x16_f16 v[32:47], v[196:199], v[100:103], v[32:47]\n"
    "v_pk_mul_f16 v98, v83, v83 op_sel_hi:[1,0]\n"
    "v_pk_mul_f16 v99, v84, v83 op_sel_hi:[1,0]\n"
    "v_add_u32_sdwa v129, v130, v122 dst_sel:DWORD dst_unused:UNUSED_PAD src0_sel:DWORD src1_sel:WORD_1\n"
    "v_mfma_f32_32x32x16_f16 v[48:63], v[108:111], v[100:103], v[48:63]\n"
    "v_pk_mul_f16 v100, v84, v83 op_sel:[0,1]\n"
    "v_pk_mul_f16 v101, v85, v83 op_sel_hi:[1,0]\n"
    "ds_read_b128 v[104:107], v131 offset:8320\n"
    "v_mfma_f32_32x32x16_f16 v[32:47], v[200:203], v[96:99], v[32:47]\n"
    "v_pk_mul_f16 v102, v85, v83 op_sel:[0,1]\n"
    "v_pk_mul_f16 v103, v86, v83 op_sel_hi:[1,0]\n"
    "v_add_u32_sdwa v128, v130, v122 dst_sel:DWORD dst_unused:UNUSED_PAD src0_sel:DWORD src1_sel:WORD_0\n"
    "v_mfma_f32_32x32x16_f16 v[48:63], v[144:147], v[96:99], v[48:63]\n"
    "v_pk_mul_f16 v96, v86, v83 op_sel:[0,1]\n"
    "v_pk_mul_f16 v97, v84, v80 op_sel_hi:[1,0]\n"
    "ds_read_b128 v[108:111], v131 offset:9360\n"
    "v_mfma_f32_32x32x16_f16 v[32:47], v[204:207], v[100:103], v[32:47]\n"
    "v_pk_mul_f16 v98, v84, v80 op_sel:[0,1]\n"
    "v_pk_mul_f16 v99, v85, v80 op_sel_hi:[1,0]\n"
    "v_fmac_f32 v135, v7, v23\n"
    "s_waitcnt lgkmcnt(2)\n"
    "v_mfma_f32_32x32x16_f16 v[48:63], v[112:115], v[100:103], v[48:63]\n"
    "v_pk_mul_f16 v100, v85, v80 op_sel:[0,1]\n"
    "v_pk_mul_f16 v101, v86, v80 op_sel_hi:[1,0]\n"
    "v_add_u32_sdwa v124, v130, v121 dst_sel:DWORD dst_unused:UNUSED_PAD src0_sel:DWORD src1_sel:WORD_0\n"
    "v_mfma_f32_32x32x16_f16 v[48:63], v[116:119], v[80:83], v[48:63]\n"
    "ds_read_b128 v[112:115], v131 offset:10400\n"
    "ds_read_b64_tr_b16 v[88:89], v128\n"
    "v_add_u32_sdwa v126, v130, v123 dst_sel:DWORD dst_unused:UNUSED_PAD src0_sel:DWORD src1_sel:WORD_0\n"
    "v_mfma_f32_32x32x16_f16 v[32:47], v[208:211], v[96:99], v[32:47]\n"
    "v_pk_mul_f16 v102, v86, v80 op_sel:[0,1]\n"
    "v_pk_mul_f16 v103, v84, v81 op_sel_hi:[1,0]\n"
    "v_fmac_f32 v135, v8, v24\n"
    "s_waitcnt lgkmcnt(2)\n"
    "v_mfma_f32_32x32x16_f16 v[48:63], v[104:107], v[96:99], v[48:63]\n"
    "v_pk_mul_f16 v96, v84, v81 op_sel:[0,1]\n"
    "v_pk_mul_f16 v97, v85, v81 op_sel_hi:[1,0]\n"
    "ds_read_b64_tr_b16 v[90:91], v129\n"
    "v_mfma_f32_32x32x16_f16 v[32:47], v[212:215], v[100:103], v[32:47]\n"
    "v_pk_mul_f16 v98, v85, v81 op_sel:[0,1]\n"
    "v_pk_mul_f16 v99, v86, v81 op_sel_hi:[1,0]\n"
    "ds_read_b128 v[116:119], v131 offset:11440\n"
    "v_mfma_f32_32x32x16_f16 v[48:63], v[148:151], v[100:103], v[48:63]\n"
    "v_pk_mul_f16 v100, v86, v81 op_sel:[0,1]\n"
    "v_pk_mul_f16 v101, v84, v82 op_sel_hi:[1,0]\n"
    "v_add_u32_sdwa v129, v130, v120 dst_sel:DWORD dst_unused:UNUSED_PAD src0_sel:DWORD src1_sel:WORD_1\n"
    "v_mfma_f32_32x32x16_f16 v[32:47], v[216:219], v[96:99], v[32:47]\n"
    "v_pk_mul_f16 v102, v84, v82 op_sel:[0,1]\n"
    "v_pk_mul_f16 v103, v85, v82 op_sel_hi:[1,0]\n"
    "v_add_u32_sdwa v128, v130, v120 dst_sel:DWORD dst_unused:UNUSED_PAD src0_sel:DWORD src1_sel:WORD_0\n"
    "v_fmac_f32 v135, v9, v25\n"
    "v_mfma_f32_32x32x16_f16 v[48:63], v[108:111], v[96:99], v[48:63]\n"
    "v_pk_mul_f16 v96, v85, v82 op_sel:[0,1]\n"
    "v_pk_mul_f16 v97, v86, v82 op_sel_hi:[1,0]\n"
    "ds_read_b128 v[104:107], v131 offset:12480\n"
    "v_mfma_f32_32x32x16_f16 v[32:47], v[220:223], v[100:103], v[32:47]\n"
    "v_pk_mul_f16 v98, v86, v82 op_sel:[0,1]\n"
    "v_pk_mul_f16 v99, v84, v85 op_sel_hi:[1,0]\n"
    "v_fmac_f32 v135, v10, v26\n"
    "s_waitcnt lgkmcnt(1)\n"
    "v_mfma_f32_32x32x16_f16 v[48:63], v[112:115], v[100:103], v[48:63]\n"
    "v_pk_mul_f16 v100, v84, v85 op_sel:[0,1]\n"
    "v_pk_mul_f16 v101, v84, v86 op_sel_hi:[1,0]\n"
    "ds_read_b128 v[112:115], v131 offset:14560\n"
    "v_mfma_f32_32x32x16_f16 v[32:47], v[224:227], v[96:99], v[32:47]\n"
    "v_pk_mul_f16 v102, v84, v86 op_sel:[0,1]\n"
    "v_pk_mul_f16 v103, v85, v86 op_sel_hi:[1,0]\n"
    "ds_read_b128 v[108:111], v131 offset:13520\n"
    "v_mfma_f32_32x32x16_f16 v[48:63], v[152:155], v[96:99], v[48:63]\n"
    "v_pk_mul_f16 v96, v85, v86 op_sel:[0,1]\n"
    "v_pk_mul_f16 v97, v84, v84 op_sel_hi:[1,0]\n"
    "ds_read_b128 v[120:123], v132 offset:512\n"
    "v_mfma_f32_32x32x16_f16 v[32:47], v[228:231], v[100:103], v[32:47]\n"
    "v_pk_mul_f16 v98, v85, v85 op_sel_hi:[1,0]\n"
    "v_pk_mul_f16 v99, v86, v86 op_sel_hi:[1,0]\n"
    "v_fmac_f32 v135, v11, v27\n"
    "v_mfma_f32_32x32x16_f16 v[48:63], v[116:119], v[100:103], v[48:63]\n"
    "v_pk_mul_f16 v100, v87, v87\n"
    "v_pk_mul_f16 v101, v84, v92 op_sel_hi:[1,0]\n"
    "v_fmac_f32 v135, v12, v28\n"
    "v_mfma_f32_32x32x16_f16 v[32:47], v[232:235], v[96:99], v[32:47]\n"
    "v_pk_mul_f16 v102, v84, v92 op_sel:[0,1]\n"
    "v_pk_mul_f16 v103, v84, v93 op_sel_hi:[1,0]\n"
    "s_waitcnt lgkmcnt(1)\n"
    "v_mfma_f32_32x32x16_f16 v[48:63], v[104:107], v[96:99], v[48:63]\n"
    "v_pk_mul_f16 v96, v84, v93 op_sel:[0,1]\n"
    "v_pk_mul_f16 v97, v84, v94 op_sel_hi:[1,0]\n"
    "ds_read_b64_tr_b16 v[82:83], v129\n"
    "v_mfma_f32_32x32x16_f16 v[48:63], v[164:167], v[84:87], v[48:63]\n"
    "ds_read_b128 v[116:119], v131 offset:15600\n"
    "ds_read_b64_tr_b16 v[80:81], v128\n"
    "v_cmp_class_f32 vcc, v135, s59\n"
    "v_mfma_f32_32x32x16_f16 v[32:47], v[236:239], v[100:103], v[32:47]\n"
    "v_pk_mul_f16 v98, v84, v94 op_sel:[0,1]\n"
    "v_pk_mul_f16 v99, v85, v92 op_sel_hi:[1,0]\n"
    "ds_read_b128 v[104:107], v131 offset:1040\n"
    "v_mfma_f32_32x32x16_f16 v[48:63], v[156:159], v[100:103], v[48:63]\n"
    "v_pk_mul_f16 v100, v85, v92 op_sel:[0,1]\n"
    "v_pk_mul_f16 v101, v85, v93 op_sel_hi:[1,0]\n"
    "s_or_b64 s[60:61], s[60:61], vcc\n"
    "v_mfma_f32_32x32x16_f16 v[32:47], v[240:243], v[96:99], v[32:47]\n"
    "v_pk_mul_f16 v102, v85, v93 op_sel:[0,1]\n"
    "v_pk_mul_f16 v103, v85, v94 op_sel_hi:[1,0]\n"
    "ds_write_b32 v134, v135\n"
    "v_mfma_f32_32x32x16_f16 v[48:63], v[108:111], v[96:99], v[48:63]\n"
    "v_pk_mul_f16 v96, v85, v94 op_sel:[0,1]\n"
    "v_pk_mul_f16 v97, v86, v92 op_sel_hi:[1,0]\n"
    "ds_read_b64_tr_b16 v[84:85], v124\n"
    "v_mfma_f32_32x32x16_f16 v[32:47], v[244:247], v[100:103], v[32:47]\n"
    "v_pk_mul_f16 v98, v86, v92 op_sel:[0,1]\n"
    "v_pk_mul_f16 v99, v86, v93 op_sel_hi:[1,0]\n"
    "ds_read_b128 v[108:111], v131 offset:2080\n"
    "s_waitcnt lgkmcnt(5)\n"
    "v_mfma_f32_32x32x16_f16 v[48:63], v[112:115], v[100:103], v[48:63]\n"
    "v_pk_mul_f16 v100, v86, v93 op_sel:[0,1]\n"
    "v_pk_mul_f16 v101, v86, v94 op_sel_hi:[1,0]\n"
    "ds_read_b64_tr_b16 v[92:93], v126\n"
    "v_mfma_f32_32x32x16_f16 v[32:47], v[248:251], v[96:99], v[32:47]\n"
    "v_pk_mul_f16 v102, v86, v94 op_sel:[0,1]\n"
    "v_pk_mul_f16 v103, v95, v95\n"
    "ds_read_b64_tr_b16 v[86:87], v125\n"
    "v_mfma_f32_32x32x16_f16 v[48:63], v[160:163], v[96:99], v[48:63]\n"
    "v_pk_mul_f16 v96, v91, v91\n"
    "s_waitcnt lgkmcnt(6)\n"
    "v_pk_mul_f16 v97, v88, v80 op_sel_hi:[1,0]\n"
    "ds_read_b128 v[112:115], v131 offset:3120\n"
    "v_mfma_f32_32x32x16_f16 v[32:47], v[252:255], v[100:103], v[32:47]\n"
    "v_pk_mul_f16 v98, v88, v80 op_sel:[0,1]\n"
    "v_pk_mul_f16 v99, v88, v81 op_sel_hi:[1,0]\n"
    "ds_read_b64_tr_b16 v[94:95], v127\n"
    "v_mfma_f32_32x32x16_f16 v[48:63], v[116:119], v[100:103], v[48:63]\n"
    "v_pk_mul_f16 v100, v88, v81 op_sel:[0,1]\n"
    "v_pk_mul_f16 v101, v88, v82 op_sel_hi:[1,0]\n"
    "ds_read_b128 v[116:119], v131 offset:4160\n"
    "Lcin_b:\n"
    "v_mfma_f32_32x32x16_f16 v[0:15], v[168:171], v[96:99], 0\n"
    "v_pk_mul_f16 v102, v88, v82 op_sel:[0,1]\n"
    "v_pk_mul_f16 v103, v89, v80 op_sel_hi:[1,0]\n"
    "s_waitcnt lgkmcnt(5)\n"
    "v_mfma_f32_32x32x16_f16 v[16:31], v[104:107], v[96:99], v[64:79]\n"
    "v_pk_mul_f16 v96, v89, v80 op_sel:[0,1]\n"
    "v_pk_mul_f16 v97, v89, v81 op_sel_hi:[1,0]\n"
    "v_mfma_f32_32x32x16_f16 v[0:15], v[172:175], v[100:103], v[0:15]\n"
    "v_pk_mul_f16 v98, v89, v81 op_sel:[0,1]\n"
    "v_pk_mul_f16 v99, v89, v82 op_sel_hi:[1,0]\n"
    "ds_read_b128 v[104:107], v131 offset:5200\n"
    "v_mfma_f32_32x32x16_f16 v[16:31], v[108:111], v[100:103], v[16:31]\n"
    "v_pk_mul_f16 v100, v89, v82 op_sel:[0,1]\n"
    "v_pk_mul_f16 v101, v90, v80 op_sel_hi:[1,0]\n"
    "ds_read_b128 v[108:111], v131 offset:6240\n"
    "v_mul_f32 v135, v32, v48\n"
    "v_mfma_f32_32x32x16_f16 v[0:15], v[176:179], v[96:99], v[0:15]\n"
    "v_pk_mul_f16 v102, v90, v80 op_sel:[0,1]\n"
    "v_pk_mul_f16 v103, v90, v81 op_sel_hi:[1,0]\n"
    "v_fmac_f32 v135, v33, v49\n"
    "v_mfma_f32_32x32x16_f16 v[16:31], v[136:139], v[96:99], v[16:31]\n"
    "v_pk_mul_f16 v96, v90, v81 op_sel:[0,1]\n"
    "v_pk_mul_f16 v97, v90, v82 op_sel_hi:[1,0]\n"
    "v_fmac_f32 v135, v34, v50\n"
    "v_mfma_f32_32x32x16_f16 v[0:15], v[180:183], v[100:103], v[0:15]\n"
    "v_pk_mul_f16 v98, v90, v82 op_sel:[0,1]\n"
    "v_pk_mul_f16 v99, v80, v81 op_sel_hi:[1,0]\n"
    "v_fmac_f32 v135, v35, v51\n"
    "s_waitcnt lgkmcnt(2)\n"
    "v_mfma_f32_32x32x16_f16 v[16:31], v[112:115], v[100:103], v[16:31]\n"
    "v_pk_mul_f16 v100, v80, v81 op_sel:[0,1]\n"
    "v_pk_mul_f16 v101, v80, v82 op_sel_hi:[1,0]\n"
    "v_add_u32_sdwa v129, v130, v122 dst_sel:DWORD dst_unused:UNUSED_PAD src0_sel:DWORD src1_sel:WORD_1\n"
    "v_fmac_f32 v135, v36, v52\n"
    "v_mfma_f32_32x32x16_f16 v[0:15], v[184:187], v[96:99], v[0:15]\n"
    "v_pk_mul_f16 v102, v80, v82 op_sel:[0,1]\n"
    "v_pk_mul_f16 v103, v81, v82 op_sel_hi:[1,0]\n"
    "ds_read_b128 v[112:115], v131 offset:7280\n"
    "v_mfma_f32_32x32x16_f16 v[16:31], v[116:119], v[96:99], v[16:31]\n"
    "v_pk_mul_f16 v96, v81, v82 op_sel:[0,1]\n"
    "v_pk_mul_f16 v97, v80, v83 op_sel_hi:[1,0]\n"
    "v_add_u32_sdwa v125, v130, v121 dst_sel:DWORD dst_unused:UNUSED_PAD src0_sel:DWORD src1_sel:WORD_1\n"
    "v_fmac_f32 v135, v37, v53\n"
    "v_mfma_f32_32x32x16_f16 v[0:15], v[188:191], v[100:103], v[0:15]\n"
    "v_pk_mul_f16 v98, v80, v83 op_sel:[0,1]\n"
    "v_pk_mul_f16 v99, v81, v83 op_sel_hi:[1,0]\n"
    "v_add_u32_sdwa v124, v130, v121 dst_sel:DWORD dst_unused:UNUSED_PAD src0_sel:DWORD src1_sel:WORD_0\n"
    "v_fmac_f32 v135, v38, v54\n"
    "v_mfma_f32_32x32x16_f16 v[16:31], v[140:143], v[100:103], v[16:31]\n"
    "v_pk_mul_f16 v100, v81, v83 op_sel:[0,1]\n"
    "v_pk_mul_f16 v101, v82, v83 op_sel_hi:[1,0]\n"
    "v_fmac_f32 v135, v39, v55\n"
    "v_mfma_f32_32x32x16_f16 v[0:15], v[192:195], v[96:99], v[0:15]\n"
    "v_pk_mul_f16 v102, v82, v83 op_sel:[0,1]\n"
    "v_pk_mul_f16 v103, v80, v80 op_sel_hi:[1,0]\n"
    "v_fmac_f32 v135, v40, v56\n"
    "s_waitcnt lgkmcnt(1)\n"
    "v_mfma_f32_32x32x16_f16 v[16:31], v[104:107], v[96:99], v[16:31]\n"
    "v_pk_mul_f16 v96, v81, v81 op_sel_hi:[1,0]\n"
    "v_pk_mul_f16 v97, v82, v82 op_sel_hi:[1,0]\n"
    "ds_read_b128 v[116:119], v131 offset:0\n"
    "v_mfma_f32_32x32x16_f16 v[0:15], v[196:199], v[100:103], v[0:15]\n"
    "v_pk_mul_f16 v98, v83, v83 op_sel_hi:[1,0]\n"
    "v_pk_mul_f16 v99, v84, v83 op_sel_hi:[1,0]\n"
    "v_add_u32_sdwa v127, v130, v123 dst_sel:DWORD dst_unused:UNUSED_PAD src0_sel:DWORD src1_sel:WORD_1\n"
    "v_mfma_f32_32x32x16_f16 v[16:31], v[108:111], v[100:103], v[16:31]\n"
    "v_pk_mul_f16 v100, v84, v83 op_sel:[0,1]\n"
    "v_pk_mul_f16 v101, v85, v83 op_sel_hi:[1,0]\n"
    "ds_read_b128 v[104:107], v131 offset:8320\n"
    "v_mfma_f32_32x32x16_f16 v[0:15], v[200:203], v[96:99], v[0:15]\n"
    "v_pk_mul_f16 v102, v85, v83 op_sel:[0,1]\n"
    "v_pk_mul_f16 v103, v86, v83 op_sel_hi:[1,0]\n"
    "v_add_u32_sdwa v128, v130, v122 dst_sel:DWORD dst_unused:UNUSED_PAD src0_sel:DWORD src1_sel:WORD_0\n"
    "v_mfma_f32_32x32x16_f16 v[16:31], v[144:147], v[96:99], v[16:31]\n"
    "v_pk_mul_f16 v96, v86, v83 op_sel:[0,1]\n"
    "v_pk_mul_f16 v97, v84, v80 op_sel_hi:[1,0]\n"
    "ds_read_b128 v[108:111], v131 offset:9360\n"
    "v_mfma_f32_32x32x16_f16 v[0:15], v[204:207], v[100:103], v[0:15]\n"
    "v_pk_mul_f16 v98, v84, v80 op_sel:[0,1]\n"
    "v_pk_mul_f16 v99, v85, v80 op_sel_hi:[1,0]\n"
    "v_fmac_f32 v135, v41, v57\n"
    "s_waitcnt lgkmcnt(2)\n"
    "v_mfma_f32_32x32x16_f16 v[16:31], v[112:115], v[100:103], v[16:31]\n"
    "v_pk_mul_f16 v100, v85, v80 op_sel:[0,1]\n"
    "v_pk_mul_f16 v101, v86, v80 op_sel_hi:[1,0]\n"
    "ds_read_b128 v[112:115], v131 offset:10400\n"
    "v_mfma_f32_32x32x16_f16 v[16:31], v[116:119], v[80:83], v[16:31]\n"
    "ds_read_b64_tr_b16 v[90:91], v129\n"
    "v_add_u32_sdwa v126, v130, v123 dst_sel:DWORD dst_unused:UNUSED_PAD src0_sel:DWORD src1_sel:WORD_0\n"
    "v_fmac_f32 v135, v42, v58\n"
    "v_mfma_f32_32x32x16_f16 v[0:15], v[208:211], v[96:99], v[0:15]\n"
    "v_pk_mul_f16 v102, v86, v80 op_sel:[0,1]\n"
    "v_pk_mul_f16 v103, v84, v81 op_sel_hi:[1,0]\n"
    "v_fmac_f32 v135, v43, v59\n"
    "s_waitcnt lgkmcnt(2)\n"
    "v_mfma_f32_32x32x16_f16 v[16:31], v[104:107], v[96:99], v[16:31]\n"
    "v_pk_mul_f16 v96, v84, v81 op_sel:[0,1]\n"
    "v_pk_mul_f16 v97, v85, v81 op_sel_hi:[1,0]\n"
    "ds_read_b128 v[116:119], v131 offset:11440\n"
    "v_mfma_f32_32x32x16_f16 v[0:15], v[212:215], v[100:103], v[0:15]\n"
    "v_pk_mul_f16 v98, v85, v81 op_sel:[0,1]\n"
    "v_pk_mul_f16 v99, v86, v81 op_sel_hi:[1,0]\n"
    "ds_read_b64_tr_b16 v[88:89], v128\n"
    "v_mfma_f32_32x32x16_f16 v[16:31], v[148:151], v[100:103], v[16:31]\n"
    "v_pk_mul_f16 v100, v86, v81 op_sel:[0,1]\n"
    "v_pk_mul_f16 v101, v84, v82 op_sel_hi:[1,0]\n"
    "v_add_u32_sdwa v129, v130, v120 dst_sel:DWORD dst_unused:UNUSED_PAD src0_sel:DWORD src1_sel:WORD_1\n"
    "v_mfma_f32_32x32x16_f16 v[0:15], v[216:219], v[96:99], v[0:15]\n"
    "v_pk_mul_f16 v102, v84, v82 op_sel:[0,1]\n"
    "v_pk_mul_f16 v103, v85, v82 op_sel_hi:[1,0]\n"
    "ds_read_b128 v[104:107], v131 offset:12480\n"
    "v_mfma_f32_32x32x16_f16 v[16:31], v[108:111], v[96:99], v[16:31]\n"
    "v_pk_mul_f16 v96, v85, v82 op_sel:[0,1]\n"
    "v_pk_mul_f16 v97, v86, v82 op_sel_hi:[1,0]\n"
    "v_add_u32_sdwa v128, v130, v120 dst_sel:DWORD dst_unused:UNUSED_PAD src0_sel:DWORD src1_sel:WORD_0\n"
    "v_mfma_f32_32x32x16_f16 v[0:15], v[220:223], v[100:103], v[0:15]\n"
    "v_pk_mul_f16 v98, v86, v82 op_sel:[0,1]\n"
    "v_pk_mul_f16 v99, v84, v85 op_sel_hi:[1,0]\n"
    "v_fmac_f32 v135, v44, v60\n"
    "s_waitcnt lgkmcnt(2)\n"
    "v_mfma_f32_32x32x16_f16 v[16:31], v[112:115], v[100:103], v[16:31]\n"
    "v_pk_mul_f16 v100, v84, v85 op_sel:[0,1]\n"
    "v_pk_mul_f16 v101, v84, v86 op_sel_hi:[1,0]\n"
    "ds_read_b128 v[108:111], v131 offset:13520\n"
    "v_mfma_f32_32x32x16_f16 v[0:15], v[224:227], v[96:99], v[0:15]\n"
    "v_pk_mul_f16 v102, v84, v86 op_sel:[0,1]\n"
    "v_pk_mul_f16 v103, v85, v86 op_sel_hi:[1,0]\n"
    "v_cmp_class_f32 vcc, v135, s59\n"
    "v_mfma_f32_32x32x16_f16 v[16:31], v[152:155], v[96:99], v[16:31]\n"
    "v_pk_mul_f16 v96, v85, v86 op_sel:[0,1]\n"
    "v_pk_mul_f16 v97, v84, v84 op_sel_hi:[1,0]\n"
    "ds_write_b32 v133, v135\n"
    "v_mfma_f32_32x32x16_f16 v[0:15], v[228:231], v[100:103], v[0:15]\n"
    "v_pk_mul_f16 v98, v85, v85 op_sel_hi:[1,0]\n"
    "v_pk_mul_f16 v99, v86, v86 op_sel_hi:[1,0]\n"
    "ds_read_b128 v[112:115], v131 offset:14560\n"
    "v_mfma_f32_32x32x16_f16 v[16:31], v[116:119], v[100:103], v[16:31]\n"
    "v_pk_mul_f16 v100, v87, v87\n"
    "v_pk_mul_f16 v101, v84, v92 op_sel_hi:[1,0]\n"
    "ds_read_b128 v[120:123], v132 offset:768\n"
    "v_mfma_f32_32x32x16_f16 v[0:15], v[232:235], v[96:99], v[0:15]\n"
    "v_pk_mul_f16 v102, v84, v92 op_sel:[0,1]\n"
    "v_pk_mul_f16 v103, v84, v93 op_sel_hi:[1,0]\n"
    "s_or_b64 s[60:61], s[60:61], vcc\n"
    "s_waitcnt lgkmcnt(3)\n"
    "v_mfma_f32_32x32x16_f16 v[16:31], v[104:107], v[96:99], v[16:31]\n"
    "v_pk_mul_f16 v96, v84, v93 op_sel:[0,1]\n"
    "v_pk_mul_f16 v97, v84, v94 op_sel_hi:[1,0]\n"
    "v_add_u32 v132, 512, v132\n"
    "v_mfma_f32_32x32x16_f16 v[16:31], v[164:167], v[84:87], v[16:31]\n"
    "ds_read_b128 v[116:119], v131 offset:15600\n"
    "v_add_u32 v134, 256, v133\n"
    "v_add_u32 v133, 512, v133\n"
    "v_mfma_f32_32x32x16_f16 v[0:15], v[236:239], v[100:103], v[0:15]\n"
    "v_pk_mul_f16 v98, v84, v94 op_sel:[0,1]\n"
    "v_pk_mul_f16 v99, v85, v92 op_sel_hi:[1,0]\n"
    "ds_read_b128 v[104:107], v131 offset:1040\n"
    "v_mfma_f32_32x32x16_f16 v[16:31], v[156:159], v[100:103], v[16:31]\n"
    "v_pk_mul_f16 v100, v85, v92 op_sel:[0,1]\n"
    "v_pk_mul_f16 v101, v85, v93 op_sel_hi:[1,0]\n"
    "ds_read_b64_tr_b16 v[82:83], v129\n"
    "v_mfma_f32_32x32x16_f16 v[0:15], v[240:243], v[96:99], v[0:15]\n"
    "v_pk_mul_f16 v102, v85, v93 op_sel:[0,1]\n"
    "v_pk_mul_f16 v103, v85, v94 op_sel_hi:[1,0]\n"
    "ds_read_b64_tr_b16 v[80:81], v128\n"
    "v_mfma_f32_32x32x16_f16 v[16:31], v[108:111], v[96:99], v[16:31]\n"
    "v_pk_mul_f16 v96, v85, v94 op_sel:[0,1]\n"
    "v_pk_mul_f16 v97, v86, v92 op_sel_hi:[1,0]\n"
    "ds_read_b64_tr_b16 v[84:85], v124\n"
    "v_mfma_f32_32x32x16_f16 v[0:15], v[244:247], v[100:103], v[0:15]\n"
    "v_pk_mul_f16 v98, v86, v92 op_sel:[0,1]\n"
    "v_pk_mul_f16 v99, v86, v93 op_sel_hi:[1,0]\n"
    "ds_read_b128 v[108:111], v131 offset:2080\n"
    "s_waitcnt lgkmcnt(5)\n"
    "v_mfma_f32_32x32x16_f16 v[16:31], v[112:115], v[100:103], v[16:31]\n"
    "v_pk_mul_f16 v100, v86, v93 op_sel:[0,1]\n"
    "v_pk_mul_f16 v101, v86, v94 op_sel_hi:[1,0]\n"
    "ds_read_b64_tr_b16 v[92:93], v126\n"
    "v_mfma_f32_32x32x16_f16 v[0:15], v[248:251], v[96:99], v[0:15]\n"
    "v_pk_mul_f16 v102, v86, v94 op_sel:[0,1]\n"
    "v_pk_mul_f16 v103, v95, v95\n"
    "ds_read_b128 v[112:115], v131 offset:3120\n"
    "v_mfma_f32_32x32x16_f16 v[16:31], v[160:163], v[96:99], v[16:31]\n"
    "v_pk_mul_f16 v96, v91, v91\n"
    "s_waitcnt lgkmcnt(4)\n"
    "v_pk_mul_f16 v97, v88, v80 op_sel_hi:[1,0]\n"
    "ds_read_b64_tr_b16 v[86:87], v125\n"
    "v_mfma_f32_32x32x16_f16 v[0:15], v[252:255], v[100:103], v[0:15]\n"
    "v_pk_mul_f16 v98, v88, v80 op_sel:[0,1]\n"
    "v_pk_mul_f16 v99, v88, v81 op_sel_hi:[1,0]\n"
    "ds_read_b64_tr_b16 v[94:95], v127\n"
    "v_mfma_f32_32x32x16_f16 v[16:31], v[116:119], v[100:103], v[16:31]\n"
    "v_pk_mul_f16 v100, v88, v81 op_sel:[0,1]\n"
    "v_pk_mul_f16 v101, v88, v82 op_sel_hi:[1,0]\n"
    "ds_read_b128 v[116:119], v131 offset:4160\n"
    "s_sub_u32 s58, s58, 1\n"
    "s_cmp_lg_u32 s58, 0\n"
    "s_cbranch_scc1 Lcin\n"
    "Lfin:\n"
    "s_lshl1_add_u32 s72, s55, s57\n"
    "v_mov_b32 v71, 44608\n"
    "s_add_i32 s72, s72, s46\n"
    "s_sub_i32 s72, 0, s72\n"
    "s_nop 1\n"
    "v_mul_f32 v135, v0, v16\n"
    "v_mul_f32 v64, v1, v17\n"
    "v_fmac_f32 v135, v2, v18\n"
    "v_fmac_f32 v64, v3, v19\n"
    "v_fmac_f32 v135, v4, v20\n"
    "v_fmac_f32 v64, v5, v21\n"
    "v_fmac_f32 v135, v6, v22\n"
    "v_fmac_f32 v64, v7, v23\n"
    "v_fmac_f32 v135, v8, v24\n"
    "v_fmac_f32 v64, v9, v25\n"
    "v_fmac_f32 v135, v10, v26\n"
    "v_fmac_f32 v64, v11, v27\n"
    "v_fmac_f32 v135, v12, v28\n"
    "v_add_f32 v135, v135, v64\n"
    "ds_write_b32 v134, v135\n"
    "v_cmp_class_f32 vcc, v135, s59\n"
    "ds_read_b128 v[72:75], v77 offset:32768\n"
    "ds_read_b128 v[64:67], v77 offset:32784\n"
    "s_or_b64 vcc, vcc, s[60:61]\n"
    "s_cbranch_vccnz Lovf\n"
    "Lovfr:\n"
    "s_barrier\n"
    "ds_read2_b32 v[68:69], v79 offset0:0 offset1:32\n"
    "ds_read2_b32 v[70:71], v71 offset0:0 offset1:4\n"
    "s_waitcnt lgkmcnt(3)\n"
    "v_add_f32 v72, v72, v73\n"
    "v_add_f32 v74, v74, v75\n"
    "s_waitcnt lgkmcnt(2)\n"
    "v_add_f32 v64, v64, v65\n"
    "v_add_f32 v66, v66, v67\n"
    "v_add_f32 v72, v72, v74\n"
    "v_add_f32 v64, v64, v66\n"
    "v_add_f32 v72, v72, v64\n"
    "v_ldexp_f32 v72, v72, s72\n"
    "s_waitcnt lgkmcnt(1)\n"
    "v_add_f32 v68, v68, v69\n"
    "v_add_f32 v72, v72, v68\n"
    "s_waitcnt lgkmcnt(0)\n"
    "v_or_b32 v70, v70, v71\n"
    "s_mov_b32 vcc_lo, 0x11111111\n"
    "v_readfirstlane_b32 s51, v70\n"
    "v_add_f32_dpp v72, v72, v72 quad_perm:[1,0,3,2] row_mask:0xf bank_mask:0xf\n"
    "s_mov_b32 vcc_hi, 0x11111111\n"
    "s_cmp_lg_u32 s51, 0\n"
    "s_cbranch_scc1 Lretry\n"
    "v_add_f32_dpp v72, v72, v72 quad_perm:[2,3,0,1] row_mask:0xf bank_mask:0xf\n"
    "s_mov_b64 exec, vcc\n"
    "v_cvt_pk_bf16_f32 v72, v72, v72\n"
    "global_store_short v78, v72, s[34:35]\n"
    "s_endpgm\n"
    "Ldnn:\n"
    "s_waitcnt lgkmcnt(0)\n"
    "s_barrier\n"
    "buffer_load_dwordx4 v40, s[12:15], s53 offen offset:0 lds\n"
    "s_mul_i32 s52, s65, 192\n"
    "v_add_u32 v41, s52, v2\n"
    "s_mul_i32 s59, s65, 7168\n"
    "v_mul_u32_u24 v39, 43691, v183\n"
    "v_lshrrev_b32 v39, 19, v39\n"
    "buffer_load_dwordx4 v40, s[12:15], s53 offen offset:1024 lds\n"
    "v_mul_u32_u24 v39, 640, v39\n"
    "v_add_u32 v162, v39, v41\n"
    "v_add_u32 v38, 64, v183\n"
    "v_mul_u32_u24 v39, 43691, v38\n"
    "v_lshrrev_b32 v39, 19, v39\n"
    "buffer_load_dwordx4 v40, s[12:15], s53 offen offset:2048 lds\n"
    "v_mul_u32_u24 v39, 640, v39\n"
    "v_add_u32 v163, v39, v41\n"
    "v_add_u32 v38, 128, v183\n"
    "v_mul_u32_u24 v39, 43691, v38\n"
    "v_lshrrev_b32 v39, 19, v39\n"
    "buffer_load_dwordx4 v40, s[12:15], s53 offen offset:3072 lds\n"
    "s_add_u32 s53, s53, 4096\n"
    "s_mov_b32 m0, s53\n"
    "v_mul_u32_u24 v39, 640, v39\n"
    "v_add_u32 v164, v39, v41\n"
    "s_add_u32 s59, s59, 45696\n"
    "v_add_u32 v38, s52, v34\n"
    "v_add_u32 v39, 576, v34\n"
    "buffer_load_dwordx4 v40, s[12:15], s53 offen offset:0 lds\n"
    "v_cmp_gt_u32 vcc, 48, v183\n"
    "s_mov_b32 s30, 832\n"
    "s_mov_b32 s31, 0x20000\n"
    "v_cndmask_b32 v38, v39, v38, vcc\n"
    "s_mov_b32 s26, 346112\n"
    "buffer_load_dwordx4 v40, s[12:15], s53 offen offset:1024 lds\n"
    "s_mov_b32 s27, 0x20000\n"
    "buffer_load_dword v169, v38, s[28:31], 0 offen\n"
    "s_mov_b32 s38, 832\n"
    "s_mov_b32 s39, 0x20000\n"
    "buffer_load_dword v170, v38, s[36:39], 0 offen\n"
    "buffer_load_dwordx4 v40, s[12:15], s53 offen offset:2048 lds\n"
    "v_lshrrev_b32 v38, 2, v183\n"
    "s_lshr_b32 s63, s65, 1\n"
    "v_mul_u32_u24 v38, 832, v38\n"
    "s_mul_i32 s62, s63, 13312\n"
    "v_lshl_add_u32 v38, v31, 4, v38\n"
    "buffer_load_dwordx4 v40, s[12:15], s53 offen offset:3072 lds\n"
    "s_add_u32 s62, s62, 768\n"
    "v_add_u32 v165, s62, v38\n"
    "v_add_u32 v189, 13312, v162\n"
    "v_add_u32 v190, 13312, v163\n"
    "v_add_u32 v191, 13312, v164\n"
    "s_mul_i32 s51, s65, 12480\n"
    "s_add_u32 s51, s51, 116000\n"
    "v_add_u32 v247, s51, v2\n"
    "s_mov_b32 exec_lo, 0xee000000\n"
    "s_mov_b32 exec_hi, 0xee000000\n"
    "v_mov_b32 v247, 0x30000\n"
    "s_mov_b64 exec, -1\n"
    "s_mov_b32 m0, s59\n"
    "s_mov_b32 s61, 0\n"
    "s_nop 0\n"
    "buffer_load_dwordx4 v162, s[24:27], s61 offen offset:0 lds\n"
    "v_mov_b32 v202, 44544\n"
    "v_mov_b32 v203, 1\n"
    "buffer_load_dwordx4 v163, s[24:27], s61 offen offset:1024 lds\n"
    "ds_read_b128 v[240:243], v247 offset:1040\n"
    "buffer_load_dwordx4 v164, s[24:27], s61 offen offset:2048 lds\n"
    "s_add_u32 m0, s59, 3072\n"
    "s_nop 0\n"
    "buffer_load_dwordx4 v189, s[24:27], s61 offen offset:0 lds\n"
    "buffer_load_dwordx4 v190, s[24:27], s61 offen offset:1024 lds\n"
    "buffer_load_dwordx4 v191, s[24:27], s61 offen offset:2048 lds\n"
    "s_add_u32 m0, s59, 6144\n"
    "s_nop 0\n"
    "buffer_load_dwordx4 v165, s[24:27], s61 offen lds\n"
    "s_and_b32 s64, s65, 1\n"
    "v_and_b32 v37, 1, v28\n"
    "v_lshl_add_u32 v36, v37, 5, v32\n"
    "v_lshl_add_u32 v35, v29, 3, v30\n"
    "s_add_u32 s60, s59, 86944\n"
    "v_mul_u32_u24 v37, 192, v35\n"
    "v_add3_u32 v160, v37, v36, s59\n"
    "s_lshl_b32 s53, s63, 5\n"
    "v_xor_b32 v37, s53, v36\n"
    "s_add_u32 s53, s59, 6144\n"
    "v_lshl_add_u32 v37, v35, 6, v37\n"
    "v_add_u32 v161, s53, v37\n"
    "v_add_u32 v187, 86944, v160\n"
    "v_add_u32 v188, 86944, v161\n"
    "s_lshl_b32 s53, s63, 7\n"
    "v_lshlrev_b32 v38, 2, v26\n"
    "s_lshl1_add_u32 s53, s64, s53\n"
    "v_add_u32 v166, 40960, v38\n"
    "v_add_u32 v167, s53, v166\n"
    "ds_read2_b32 v[174:175], v166 offset0:0 offset1:32\n"
    "ds_read_u16 v173, v167\n"
    "v_lshlrev_b32 v168, 4, v29\n"
    "s_lshl_b32 s53, s65, 9\n"
    "s_add_u32 s53, s53, 74368\n"
    "v_add_u32 v171, s53, v168\n"
    "v_lshl_add_u32 v182, v183, 3, s53\n"
    "s_add_u32 s53, s53, 384\n"
    "s_cmp_eq_u32 s63, 0\n"
    "s_cselect_b32 s53, s53, 44416\n"
    "v_add_u32 v172, s53, v168\n"
    "s_cmp_eq_u32 s64, 0\n"
    "s_cselect_b32 s66, 1.0, 0\n"
    "s_cselect_b32 s67, 0, 1.0\n"
    "s_setprio 2\n"
    "s_waitcnt vmcnt(10)\n"
    "v_lshlrev_b32 v200, 16, v169\n"
    "v_and_b32 v201, 0xffff0000, v169\n"
    "ds_write_b64 v182, v[200:201]\n"
    "ds_read_b128 v[0:3], v171 offset:0\n"
    "ds_read_b128 v[4:7], v171 offset:32\n"
    "ds_read_b128 v[8:11], v171 offset:64\n"
    "ds_read_b128 v[12:15], v171 offset:96\n"
    "ds_read_b128 v[32:35], v171 offset:128\n"
    "ds_read_b128 v[36:39], v171 offset:160\n"
    "ds_read_b128 v[40:43], v171 offset:192\n"
    "ds_read_b128 v[44:47], v171 offset:224\n"
    "ds_read_b128 v[64:67], v171 offset:256\n"
    "ds_read_b128 v[68:71], v171 offset:288\n"
    "ds_read_b128 v[72:75], v171 offset:320\n"
    "ds_read_b128 v[76:79], v171 offset:352\n"
    "ds_read_b128 v[96:99], v172 offset:0\n"
    "ds_read_b128 v[100:103], v172 offset:32\n"
    "ds_read_b128 v[104:107], v172 offset:64\n"
    "ds_read_b128 v[108:111], v172 offset:96\n"
    "s_waitcnt lgkmcnt(15)\n"
    "v_xor_b32_sdwa v176, v168, v174 dst_sel:DWORD dst_unused:UNUSED_PAD src0_sel:DWORD src1_sel:WORD_0\n"
    "v_xor_b32_sdwa v177, v168, v174 dst_sel:DWORD dst_unused:UNUSED_PAD src0_sel:DWORD src1_sel:WORD_1\n"
    "s_cmp_lg_u32 s47, 0\n"
    "s_cbranch_scc1 Lscan\n"
    "Lscanr:\n"
    "s_waitcnt vmcnt(7)\n"
    "s_mov_b64 exec, 1\n"
    "ds_add_u32 v202, v203 offset:16\n"
    "s_mov_b64 exec, -1\n"
    "s_waitcnt vmcnt(4)\n"
    "ds_read_b64_tr_b16 v[112:113], v160 offset:0\n"
    "ds_read_b64_tr_b16 v[114:115], v160 offset:768\n"
    "ds_read_b64_tr_b16 v[116:117], v160 offset:64\n"
    "ds_read_b64_tr_b16 v[118:119], v160 offset:832\n"
    "ds_read_b64_tr_b16 v[120:121], v160 offset:128\n"
    "ds_read_b64_tr_b16 v[122:123], v160 offset:896\n"
    "s_mov_b32 s58, 0x40000\n"
    "s_branch Lsp_in\n"
    "Lsp:\n"
    "s_sleep 1\n"
    "Lsp_in:\n"
    "ds_read_b64 v[204:205], v202 offset:16\n"
    "s_sub_u32 s58, s58, 1\n"
    "s_cbranch_scc1 Lsp_done\n"
    "s_waitcnt lgkmcnt(0)\n"
    "v_add_u32 v204, v204, v205\n"
    "s_nop 0\n"
    "v_readfirstlane_b32 s52, v204\n"
    "s_nop 0\n"
    "s_cmp_eq_u32 s52, 8\n"
    "s_cbranch_scc0 Lsp\n"
    "Lsp_done:\n"
    "ds_read_b128 v[140:143], v176\n"
    "ds_read_b128 v[144:147], v177\n"
    "s_mov_b32 s58, 0\n"
    "s_waitcnt lgkmcnt(0)\n"
    "v_mfma_f32_32x32x16_bf16 v[16:31], v[112:115], v[144:147], v[0:15]\n"
    "v_xor_b32_sdwa v176, v168, v175 dst_sel:DWORD dst_unused:UNUSED_PAD src0_sel:DWORD src1_sel:WORD_0\n"
    "v_xor_b32_sdwa v177, v168, v175 dst_sel:DWORD dst_unused:UNUSED_PAD src0_sel:DWORD src1_sel:WORD_1\n"
    "ds_read_b128 v[148:151], v176\n"
    "ds_read_b128 v[152:155], v177\n"
    "v_mfma_f32_32x32x16_bf16 v[0:15], v[112:115], v[140:143], v[0:15]\n"
    "s_waitcnt vmcnt(0)\n"
    "ds_read_b64_tr_b16 v[124:125], v160 offset:3072\n"
    "ds_read_b64_tr_b16 v[126:127], v160 offset:3840\n"
    "ds_read_b64_tr_b16 v[128:129], v160 offset:3136\n"
    "v_mfma_f32_32x32x16_bf16 v[48:63], v[116:119], v[144:147], v[32:47]\n"
    "ds_read_b64_tr_b16 v[130:131], v160 offset:3904\n"
    "ds_read_b64_tr_b16 v[132:133], v160 offset:3200\n"
    "ds_read_b64_tr_b16 v[134:135], v160 offset:3968\n"
    "s_mov_b32 s61, 26624\n"
    "s_add_u32 m0, s60, 0\n"
    "s_nop 0\n"
    "buffer_load_dwordx4 v162, s[24:27], s61 offen offset:0 lds\n"
    "buffer_load_dwordx4 v163, s[24:27], s61 offen offset:1024 lds\n"
    "buffer_load_dwordx4 v164, s[24:27], s61 offen offset:2048 lds\n"
    "s_add_u32 m0, s60, 3072\n"
    "s_nop 0\n"
    "buffer_load_dwordx4 v189, s[24:27], s61 offen offset:0 lds\n"
    "buffer_load_dwordx4 v190, s[24:27], s61 offen offset:1024 lds\n"
    "buffer_load_dwordx4 v191, s[24:27], s61 offen offset:2048 lds\n"
    "s_add_u32 m0, s60, 6144\n"
    "s_nop 0\n"
    "buffer_load_dwordx4 v165, s[24:27], s61 offen lds\n"
    "s_mov_b32 s61, 53248\n"
    "s_mov_b32 m0, s59\n"
    "v_mfma_f32_32x32x16_bf16 v[32:47], v[116:119], v[140:143], v[32:47]\n"
    "v_xor_b32 v178, v168, v173\n"
    "buffer_load_dwordx4 v162, s[24:27], s61 offen offset:0 lds\n"
    "ds_read2_b32 v[174:175], v166 offset0:64 offset1:96\n"
    "v_mfma_f32_32x32x16_bf16 v[80:95], v[120:123], v[144:147], v[64:79]\n"
    "buffer_load_dwordx4 v163, s[24:27], s61 offen offset:1024 lds\n"
    "ds_read_b64_tr_b16 v[136:137], v161\n"
    "ds_read_b64_tr_b16 v[138:139], v161 offset:256\n"
    "v_mfma_f32_32x32x16_bf16 v[64:79], v[120:123], v[140:143], v[64:79]\n"
    "buffer_load_dwordx4 v164, s[24:27], s61 offen offset:2048 lds\n"
    "ds_read_b128 v[156:159], v178\n"
    "s_waitcnt lgkmcnt(8)\n"
    "v_mfma_f32_32x32x16_bf16 v[0:15], v[124:127], v[148:151], v[0:15]\n"
    "s_waitcnt lgkmcnt(3)\n"
    "v_xor_b32_sdwa v176, v168, v174 dst_sel:DWORD dst_unused:UNUSED_PAD src0_sel:DWORD src1_sel:WORD_0\n"
    "v_xor_b32_sdwa v177, v168, v174 dst_sel:DWORD dst_unused:UNUSED_PAD src0_sel:DWORD src1_sel:WORD_1\n"
    "ds_read_b128 v[140:143], v176\n"
    "ds_read_b128 v[144:147], v177\n"
    "v_mfma_f32_32x32x16_bf16 v[16:31], v[124:127], v[152:155], v[16:31]\n"
    "s_waitcnt vmcnt(7)\n"
    "ds_read_b64_tr_b16 v[112:113], v187 offset:0\n"
    "ds_read_b64_tr_b16 v[114:115], v187 offset:768\n"
    "ds_read_b64_tr_b16 v[116:117], v187 offset:64\n"
    "v_mfma_f32_32x32x16_bf16 v[32:47], v[128:131], v[148:151], v[32:47]\n"
    "ds_read_b64_tr_b16 v[118:119], v187 offset:832\n"
    "ds_read_b64_tr_b16 v[120:121], v187 offset:128\n"
    "ds_read_b64_tr_b16 v[122:123], v187 offset:896\n"
    "s_add_u32 m0, s59, 3072\n"
    "v_mfma_f32_32x32x16_bf16 v[48:63], v[128:131], v[152:155], v[48:63]\n"
    "s_waitcnt lgkmcnt(9)\n"
    "buffer_load_dwordx4 v189, s[24:27], s61 offen offset:0 lds\n"
    "ds_read_u16 v173, v167 offset:256\n"
    "v_mfma_f32_32x32x16_bf16 v[64:79], v[132:135], v[148:151], v[64:79]\n"
    "buffer_load_dwordx4 v190, s[24:27], s61 offen offset:1024 lds\n"
    "v_add_u32 v166, 256, v166\n"
    "v_add_u32 v167, 256, v167\n"
    "s_waitcnt lgkmcnt(9)\n"
    "v_mfma_f32_32x32x16_bf16 v[96:111], v[136:139], v[156:159], v[96:111]\n"
    "buffer_load_dwordx4 v191, s[24:27], s61 offen offset:2048 lds\n"
    "s_add_u32 m0, s59, 6144\n"
    "v_mfma_f32_32x32x16_bf16 v[80:95], v[132:135], v[152:155], v[80:95]\n"
    "buffer_load_dwordx4 v165, s[24:27], s61 offen lds\n"
    "s_add_u32 s61, s61, 26624\n"
    "Ldnnl:\n"
    "s_waitcnt lgkmcnt(5)\n"
    "v_mfma_f32_32x32x16_bf16 v[0:15], v[112:115], v[140:143], v[0:15]\n"
    "v_xor_b32_sdwa v176, v168, v175 dst_sel:DWORD dst_unused:UNUSED_PAD src0_sel:DWORD src1_sel:WORD_0\n"
    "v_xor_b32_sdwa v177, v168, v175 dst_sel:DWORD dst_unused:UNUSED_PAD src0_sel:DWORD src1_sel:WORD_1\n"
    "ds_read_b128 v[148:151], v176\n"
    "ds_read_b128 v[152:155], v177\n"
    "v_mfma_f32_32x32x16_bf16 v[16:31], v[112:115], v[144:147], v[16:31]\n"
    "s_waitcnt vmcnt(7)\n"
    "ds_read_b64_tr_b16 v[124:125], v187 offset:3072\n"
    "ds_read_b64_tr_b16 v[126:127], v187 offset:3840\n"
    "ds_read_b64_tr_b16 v[128:129], v187 offset:3136\n"
    "s_waitcnt lgkmcnt(8)\n"
    "v_mfma_f32_32x32x16_bf16 v[32:47], v[116:119], v[140:143], v[32:47]\n"
    "ds_read_b64_tr_b16 v[130:131], v187 offset:3904\n"
    "ds_read_b64_tr_b16 v[132:133], v187 offset:3200\n"
    "ds_read_b64_tr_b16 v[134:135], v187 offset:3968\n"
    "s_mov_b32 m0, s60\n"
    "v_mfma_f32_32x32x16_bf16 v[48:63], v[116:119], v[144:147], v[48:63]\n"
    "s_waitcnt lgkmcnt(8)\n"
    "v_xor_b32 v178, v168, v173\n"
    "buffer_load_dwordx4 v162, s[24:27], s61 offen offset:0 lds\n"
    "ds_read2_b32 v[174:175], v166 offset0:64 offset1:96\n"
    "v_mfma_f32_32x32x16_bf16 v[64:79], v[120:123], v[140:143], v[64:79]\n"
    "buffer_load_dwordx4 v163, s[24:27], s61 offen offset:1024 lds\n"
    "ds_read_b64_tr_b16 v[136:137], v188\n"
    "ds_read_b64_tr_b16 v[138:139], v188 offset:256\n"
    "v_mfma_f32_32x32x16_bf16 v[80:95], v[120:123], v[144:147], v[80:95]\n"
    "buffer_load_dwordx4 v164, s[24:27], s61 offen offset:2048 lds\n"
    "ds_read_b128 v[156:159], v178\n"
    "s_waitcnt lgkmcnt(8)\n"
    "v_mfma_f32_32x32x16_bf16 v[0:15], v[124:127], v[148:151], v[0:15]\n"
    "s_waitcnt lgkmcnt(3)\n"
    "v_xor_b32_sdwa v176, v168, v174 dst_sel:DWORD dst_unused:UNUSED_PAD src0_sel:DWORD src1_sel:WORD_0\n"
    "v_xor_b32_sdwa v177, v168, v174 dst_sel:DWORD dst_unused:UNUSED_PAD src0_sel:DWORD src1_sel:WORD_1\n"
    "ds_read_b128 v[140:143], v176\n"
    "ds_read_b128 v[144:147], v177\n"
    "v_mfma_f32_32x32x16_bf16 v[16:31], v[124:127], v[152:155], v[16:31]\n"
    "s_waitcnt vmcnt(7)\n"
    "ds_read_b64_tr_b16 v[112:113], v160 offset:0\n"
    "ds_read_b64_tr_b16 v[114:115], v160 offset:768\n"
    "ds_read_b64_tr_b16 v[116:117], v160 offset:64\n"
    "v_mfma_f32_32x32x16_bf16 v[32:47], v[128:131], v[148:151], v[32:47]\n"
    "ds_read_b64_tr_b16 v[118:119], v160 offset:832\n"
    "ds_read_b64_tr_b16 v[120:121], v160 offset:128\n"
    "ds_read_b64_tr_b16 v[122:123], v160 offset:896\n"
    "s_add_u32 m0, s60, 3072\n"
    "v_mfma_f32_32x32x16_bf16 v[48:63], v[128:131], v[152:155], v[48:63]\n"
    "s_waitcnt lgkmcnt(9)\n"
    "buffer_load_dwordx4 v189, s[24:27], s61 offen offset:0 lds\n"
    "ds_read_u16 v173, v167 offset:256\n"
    "v_mfma_f32_32x32x16_bf16 v[64:79], v[132:135], v[148:151], v[64:79]\n"
    "buffer_load_dwordx4 v190, s[24:27], s61 offen offset:1024 lds\n"
    "s_waitcnt lgkmcnt(9)\n"
    "v_mfma_f32_32x32x16_bf16 v[96:111], v[136:139], v[156:159], v[96:111]\n"
    "buffer_load_dwordx4 v191, s[24:27], s61 offen offset:2048 lds\n"
    "s_add_u32 m0, s60, 6144\n"
    "v_mfma_f32_32x32x16_bf16 v[80:95], v[132:135], v[152:155], v[80:95]\n"
    "buffer_load_dwordx4 v165, s[24:27], s61 offen lds\n"
    "s_add_u32 s61, s61, 26624\n"
    "s_waitcnt lgkmcnt(5)\n"
    "v_mfma_f32_32x32x16_bf16 v[0:15], v[112:115], v[140:143], v[0:15]\n"
    "v_xor_b32_sdwa v176, v168, v175 dst_sel:DWORD dst_unused:UNUSED_PAD src0_sel:DWORD src1_sel:WORD_0\n"
    "v_xor_b32_sdwa v177, v168, v175 dst_sel:DWORD dst_unused:UNUSED_PAD src0_sel:DWORD src1_sel:WORD_1\n"
    "ds_read_b128 v[148:151], v176\n"
    "ds_read_b128 v[152:155], v177\n"
    "v_mfma_f32_32x32x16_bf16 v[16:31], v[112:115], v[144:147], v[16:31]\n"
    "s_waitcnt vmcnt(7)\n"
    "ds_read_b64_tr_b16 v[124:125], v160 offset:3072\n"
    "ds_read_b64_tr_b16 v[126:127], v160 offset:3840\n"
    "ds_read_b64_tr_b16 v[128:129], v160 offset:3136\n"
    "s_waitcnt lgkmcnt(8)\n"
    "v_mfma_f32_32x32x16_bf16 v[32:47], v[116:119], v[140:143], v[32:47]\n"
    "ds_read_b64_tr_b16 v[130:131], v160 offset:3904\n"
    "ds_read_b64_tr_b16 v[132:133], v160 offset:3200\n"
    "ds_read_b64_tr_b16 v[134:135], v160 offset:3968\n"
    "s_mov_b32 m0, s59\n"
    "v_mfma_f32_32x32x16_bf16 v[48:63], v[116:119], v[144:147], v[48:63]\n"
    "s_waitcnt lgkmcnt(8)\n"
    "v_xor_b32 v178, v168, v173\n"
    "buffer_load_dwordx4 v162, s[24:27], s61 offen offset:0 lds\n"
    "ds_read2_b32 v[174:175], v166 offset0:128 offset1:160\n"
    "v_mfma_f32_32x32x16_bf16 v[64:79], v[120:123], v[140:143], v[64:79]\n"
    "buffer_load_dwordx4 v163, s[24:27], s61 offen offset:1024 lds\n"
    "ds_read_b64_tr_b16 v[136:137], v161\n"
    "ds_read_b64_tr_b16 v[138:139], v161 offset:256\n"
    "v_mfma_f32_32x32x16_bf16 v[80:95], v[120:123], v[144:147], v[80:95]\n"
    "buffer_load_dwordx4 v164, s[24:27], s61 offen offset:2048 lds\n"
    "ds_read_b128 v[156:159], v178\n"
    "s_waitcnt lgkmcnt(8)\n"
    "v_mfma_f32_32x32x16_bf16 v[0:15], v[124:127], v[148:151], v[0:15]\n"
    "s_waitcnt lgkmcnt(3)\n"
    "v_xor_b32_sdwa v176, v168, v174 dst_sel:DWORD dst_unused:UNUSED_PAD src0_sel:DWORD src1_sel:WORD_0\n"
    "v_xor_b32_sdwa v177, v168, v174 dst_sel:DWORD dst_unused:UNUSED_PAD src0_sel:DWORD src1_sel:WORD_1\n"
    "ds_read_b128 v[140:143], v176\n"
    "ds_read_b128 v[144:147], v177\n"
    "v_mfma_f32_32x32x16_bf16 v[16:31], v[124:127], v[152:155], v[16:31]\n"
    "s_waitcnt vmcnt(7)\n"
    "ds_read_b64_tr_b16 v[112:113], v187 offset:0\n"
    "ds_read_b64_tr_b16 v[114:115], v187 offset:768\n"
    "ds_read_b64_tr_b16 v[116:117], v187 offset:64\n"
    "v_mfma_f32_32x32x16_bf16 v[32:47], v[128:131], v[148:151], v[32:47]\n"
    "ds_read_b64_tr_b16 v[118:119], v187 offset:832\n"
    "ds_read_b64_tr_b16 v[120:121], v187 offset:128\n"
    "ds_read_b64_tr_b16 v[122:123], v187 offset:896\n"
    "s_add_u32 m0, s59, 3072\n"
    "v_mfma_f32_32x32x16_bf16 v[48:63], v[128:131], v[152:155], v[48:63]\n"
    "s_waitcnt lgkmcnt(9)\n"
    "buffer_load_dwordx4 v189, s[24:27], s61 offen offset:0 lds\n"
    "ds_read_u16 v173, v167 offset:512\n"
    "v_mfma_f32_32x32x16_bf16 v[64:79], v[132:135], v[148:151], v[64:79]\n"
    "buffer_load_dwordx4 v190, s[24:27], s61 offen offset:1024 lds\n"
    "v_add_u32 v166, 512, v166\n"
    "v_add_u32 v167, 512, v167\n"
    "s_waitcnt lgkmcnt(9)\n"
    "v_mfma_f32_32x32x16_bf16 v[96:111], v[136:139], v[156:159], v[96:111]\n"
    "buffer_load_dwordx4 v191, s[24:27], s61 offen offset:2048 lds\n"
    "s_add_u32 m0, s59, 6144\n"
    "s_add_u32 s58, s58, 1\n"
    "v_mfma_f32_32x32x16_bf16 v[80:95], v[132:135], v[152:155], v[80:95]\n"
    "buffer_load_dwordx4 v165, s[24:27], s61 offen lds\n"
    "s_add_u32 s61, s61, 26624\n"
    "s_cmp_lg_u32 s58, 5\n"
    "s_cbranch_scc1 Ldnnl\n"
    "s_waitcnt lgkmcnt(5)\n"
    "v_mfma_f32_32x32x16_bf16 v[0:15], v[112:115], v[140:143], v[0:15]\n"
    "v_pk_maximum3_f16 v244, v240, v240, v240 neg_lo:[0,0,1] neg_hi:[0,0,1]\n"
    "v_xor_b32_sdwa v176, v168, v175 dst_sel:DWORD dst_unused:UNUSED_PAD src0_sel:DWORD src1_sel:WORD_0\n"
    "v_xor_b32_sdwa v177, v168, v175 dst_sel:DWORD dst_unused:UNUSED_PAD src0_sel:DWORD src1_sel:WORD_1\n"
    "ds_read_b128 v[148:151], v176\n"
    "ds_read_b128 v[152:155], v177\n"
    "v_mfma_f32_32x32x16_bf16 v[16:31], v[112:115], v[144:147], v[16:31]\n"
    "v_pk_maximum3_f16 v244, v244, v241, v241 neg_lo:[0,0,1] neg_hi:[0,0,1]\n"
    "s_waitcnt vmcnt(7)\n"
    "ds_read_b64_tr_b16 v[124:125], v187 offset:3072\n"
    "ds_read_b64_tr_b16 v[126:127], v187 offset:3840\n"
    "ds_read_b64_tr_b16 v[128:129], v187 offset:3136\n"
    "s_waitcnt lgkmcnt(8)\n"
    "v_mfma_f32_32x32x16_bf16 v[32:47], v[116:119], v[140:143], v[32:47]\n"
    "v_pk_maximum3_f16 v244, v244, v242, v242 neg_lo:[0,0,1] neg_hi:[0,0,1]\n"
    "ds_read_b64_tr_b16 v[130:131], v187 offset:3904\n"
    "ds_read_b64_tr_b16 v[132:133], v187 offset:3200\n"
    "ds_read_b64_tr_b16 v[134:135], v187 offset:3968\n"
    "v_mfma_f32_32x32x16_bf16 v[48:63], v[116:119], v[144:147], v[48:63]\n"
    "v_pk_maximum3_f16 v244, v244, v243, v243 neg_lo:[0,0,1] neg_hi:[0,0,1]\n"
    "s_waitcnt lgkmcnt(8)\n"
    "v_xor_b32 v178, v168, v173\n"
    "ds_read2_b32 v[174:175], v166 offset0:64 offset1:96\n"
    "v_mfma_f32_32x32x16_bf16 v[64:79], v[120:123], v[140:143], v[64:79]\n"
    "s_lshl_b32 s52, s63, 2\n"
    "ds_read_b64_tr_b16 v[136:137], v188\n"
    "ds_read_b64_tr_b16 v[138:139], v188 offset:256\n"
    "v_mfma_f32_32x32x16_bf16 v[80:95], v[120:123], v[144:147], v[80:95]\n"
    "v_add_u32 v246, s52, v202\n"
    "ds_read_b128 v[156:159], v178\n"
    "s_waitcnt lgkmcnt(8)\n"
    "v_mfma_f32_32x32x16_bf16 v[0:15], v[124:127], v[148:151], v[0:15]\n"
    "v_lshrrev_b32 v245, 16, v244\n"
    "s_waitcnt lgkmcnt(3)\n"
    "v_xor_b32_sdwa v176, v168, v174 dst_sel:DWORD dst_unused:UNUSED_PAD src0_sel:DWORD src1_sel:WORD_0\n"
    "v_xor_b32_sdwa v177, v168, v174 dst_sel:DWORD dst_unused:UNUSED_PAD src0_sel:DWORD src1_sel:WORD_1\n"
    "ds_read_b128 v[140:143], v176\n"
    "ds_read_b128 v[144:147], v177\n"
    "v_mfma_f32_32x32x16_bf16 v[16:31], v[124:127], v[152:155], v[16:31]\n"
    "v_and_b32 v244, 0x7fff, v244\n"
    "s_waitcnt vmcnt(4)\n"
    "ds_read_b64_tr_b16 v[112:113], v160 offset:0\n"
    "ds_read_b64_tr_b16 v[114:115], v160 offset:768\n"
    "ds_read_b64_tr_b16 v[116:117], v160 offset:64\n"
    "v_mfma_f32_32x32x16_bf16 v[32:47], v[128:131], v[148:151], v[32:47]\n"
    "v_and_b32 v245, 0x7fff, v245\n"
    "ds_read_b64_tr_b16 v[118:119], v160 offset:832\n"
    "ds_read_b64_tr_b16 v[120:121], v160 offset:128\n"
    "ds_read_b64_tr_b16 v[122:123], v160 offset:896\n"
    "v_mfma_f32_32x32x16_bf16 v[48:63], v[128:131], v[152:155], v[48:63]\n"
    "v_max_u32 v244, v244, v245\n"
    "ds_read_u16 v173, v167 offset:256\n"
    "v_mfma_f32_32x32x16_bf16 v[64:79], v[132:135], v[148:151], v[64:79]\n"
    "v_max_u32_dpp v244, v244, v244 row_shr:1 row_mask:0xf bank_mask:0xf\n"
    "s_waitcnt lgkmcnt(9)\n"
    "v_mfma_f32_32x32x16_bf16 v[96:111], v[136:139], v[156:159], v[96:111]\n"
    "v_max_u32_dpp v248, v248, v248 row_shr:1 row_mask:0xf bank_mask:0xf\n"
    "v_mfma_f32_32x32x16_bf16 v[80:95], v[132:135], v[152:155], v[80:95]\n"
    "v_max_u32_dpp v244, v244, v244 row_shr:2 row_mask:0xf bank_mask:0xf\n"
    "s_waitcnt lgkmcnt(5)\n"
    "v_mfma_f32_32x32x16_bf16 v[0:15], v[112:115], v[140:143], v[0:15]\n"
    "v_max_u32_dpp v248, v248, v248 row_shr:2 row_mask:0xf bank_mask:0xf\n"
    "v_xor_b32_sdwa v176, v168, v175 dst_sel:DWORD dst_unused:UNUSED_PAD src0_sel:DWORD src1_sel:WORD_0\n"
    "v_xor_b32_sdwa v177, v168, v175 dst_sel:DWORD dst_unused:UNUSED_PAD src0_sel:DWORD src1_sel:WORD_1\n"
    "ds_read_b128 v[148:151], v176\n"
    "ds_read_b128 v[152:155], v177\n"
    "v_mfma_f32_32x32x16_bf16 v[16:31], v[112:115], v[144:147], v[16:31]\n"
    "v_max_u32_dpp v244, v244, v244 row_shr:4 row_mask:0xf bank_mask:0xf\n"
    "s_waitcnt vmcnt(0)\n"
    "ds_read_b64_tr_b16 v[124:125], v160 offset:3072\n"
    "ds_read_b64_tr_b16 v[126:127], v160 offset:3840\n"
    "ds_read_b64_tr_b16 v[128:129], v160 offset:3136\n"
    "s_waitcnt lgkmcnt(8)\n"
    "v_mfma_f32_32x32x16_bf16 v[32:47], v[116:119], v[140:143], v[32:47]\n"
    "v_max_u32_dpp v248, v248, v248 row_shr:4 row_mask:0xf bank_mask:0xf\n"
    "ds_read_b64_tr_b16 v[130:131], v160 offset:3904\n"
    "ds_read_b64_tr_b16 v[132:133], v160 offset:3200\n"
    "ds_read_b64_tr_b16 v[134:135], v160 offset:3968\n"
    "v_mfma_f32_32x32x16_bf16 v[48:63], v[116:119], v[144:147], v[48:63]\n"
    "v_max_u32_dpp v244, v244, v244 row_shr:8 row_mask:0xf bank_mask:0xf\n"
    "s_waitcnt lgkmcnt(8)\n"
    "v_xor_b32 v178, v168, v173\n"
    "v_mfma_f32_32x32x16_bf16 v[64:79], v[120:123], v[140:143], v[64:79]\n"
    "v_max_u32_dpp v248, v248, v248 row_shr:8 row_mask:0xf bank_mask:0xf\n"
    "ds_read_b64_tr_b16 v[136:137], v161\n"
    "ds_read_b64_tr_b16 v[138:139], v161 offset:256\n"
    "v_mfma_f32_32x32x16_bf16 v[80:95], v[120:123], v[144:147], v[80:95]\n"
    "s_mov_b32 exec_lo, 0x80008000\n"
    "s_mov_b32 exec_hi, 0x80008000\n"
    "ds_max_u32 v246, v244 offset:48\n"
    "ds_max_u32 v202, v248 offset:56\n"
    "s_mov_b64 exec, 1\n"
    "ds_add_u32 v202, v203 offset:60\n"
    "s_mov_b64 exec, -1\n"
    "ds_read_b128 v[156:159], v178\n"
    "s_waitcnt lgkmcnt(10)\n"
    "v_mfma_f32_32x32x16_bf16 v[0:15], v[124:127], v[148:151], v[0:15]\n"
    "v_mfma_f32_32x32x16_bf16 v[16:31], v[124:127], v[152:155], v[16:31]\n"
    "s_waitcnt lgkmcnt(8)\n"
    "v_mfma_f32_32x32x16_bf16 v[32:47], v[128:131], v[148:151], v[32:47]\n"
    "v_mfma_f32_32x32x16_bf16 v[48:63], v[128:131], v[152:155], v[48:63]\n"
    "s_waitcnt lgkmcnt(6)\n"
    "v_mfma_f32_32x32x16_bf16 v[64:79], v[132:135], v[148:151], v[64:79]\n"
    "s_waitcnt lgkmcnt(0)\n"
    "v_mfma_f32_32x32x16_bf16 v[96:111], v[136:139], v[156:159], v[96:111]\n"
    "v_mfma_f32_32x32x16_bf16 v[80:95], v[132:135], v[152:155], v[80:95]\n"
    "s_setprio 0\n"
    "s_mov_b32 s42, 2\n"
    "s_mov_b32 s43, 0x20000\n"
    "buffer_load_ushort v112, off, s[40:43], 0\n"
    "v_lshlrev_b32 v200, 16, v170\n"
    "v_and_b32 v201, 0xffff0000, v170\n"
    "ds_write_b64 v182, v[200:201] offset:2048\n"
    "s_mul_i32 s51, s65, 7168\n"
    "s_add_u32 s51, s51, 45696\n"
    "v_lshlrev_b32 v202, 4, v183\n"
    "v_add_u32 v203, s51, v202\n"
    "ds_write_b128 v203, v[104:107] offset:1024\n"
    "ds_write_b128 v203, v[108:111] offset:2048\n"
    "s_lshl_b32 s52, s65, 2\n"
    "v_mov_b32 v204, s52\n"
    "v_mov_b32 v205, 1\n"
    "s_waitcnt lgkmcnt(0)\n"
    "ds_write_b32 v204, v205 offset:44544\n"
    "v_mov_b32 v178, 0\n"
    "v_mov_b32 v206, 0xae97ae97\n"
    "v_mov_b32 v207, 0xc09bc09b\n"
    "ds_read_b128 v[184:187], v171 offset:2048\n"
    "ds_read_b128 v[188:191], v171 offset:2080\n"
    "ds_read_b128 v[192:195], v171 offset:2112\n"
    "ds_read_b128 v[196:199], v171 offset:2144\n"
    "v_mov_b32 v179, 0\n"
    "v_cvt_pk_f16_f32 v208, v0, v1\n"
    "v_cvt_pk_f16_f32 v209, v2, v3\n"
    "v_cvt_pk_f16_f32 v210, v4, v5\n"
    "v_cvt_pk_f16_f32 v211, v6, v7\n"
    "v_cvt_pk_f16_f32 v212, v8, v9\n"
    "v_cvt_pk_f16_f32 v213, v10, v11\n"
    "v_cvt_pk_f16_f32 v214, v12, v13\n"
    "v_cvt_pk_f16_f32 v215, v14, v15\n"
    "v_pk_mul_f16 v216, v208, v208\n"
    "v_pk_mul_f16 v217, v209, v209\n"
    "v_pk_mul_f16 v218, v210, v210\n"
    "v_pk_mul_f16 v219, v211, v211\n"
    "v_pk_mul_f16 v220, v212, v212\n"
    "v_pk_mul_f16 v221, v213, v213\n"
    "v_pk_mul_f16 v222, v214, v214\n"
    "v_pk_mul_f16 v223, v215, v215\n"
    "v_pk_fma_f16 v216, v216, v206, v207\n"
    "v_pk_fma_f16 v217, v217, v206, v207\n"
    "v_pk_fma_f16 v218, v218, v206, v207\n"
    "v_pk_fma_f16 v219, v219, v206, v207\n"
    "v_pk_fma_f16 v220, v220, v206, v207\n"
    "v_pk_fma_f16 v221, v221, v206, v207\n"
    "v_pk_fma_f16 v222, v222, v206, v207\n"
    "v_pk_fma_f16 v223, v223, v206, v207\n"
    "v_pk_mul_f16 v208, v208, v216\n"
    "v_pk_mul_f16 v209, v209, v217\n"
    "v_pk_mul_f16 v210, v210, v218\n"
    "v_pk_mul_f16 v211, v211, v219\n"
    "v_pk_mul_f16 v212, v212, v220\n"
    "v_pk_mul_f16 v213, v213, v221\n"
    "v_pk_mul_f16 v214, v214, v222\n"
    "v_pk_mul_f16 v215, v215, v223\n"
    "v_exp_f16 v216, v208\n"
    "s_waitcnt lgkmcnt(3)\n"
    "v_mul_f32 v0, v0, v184\n"
    "v_exp_f16 v217, v209\n"
    "v_mul_f32 v2, v2, v186\n"
    "v_exp_f16 v218, v210\n"
    "s_waitcnt lgkmcnt(2)\n"
    "v_mul_f32 v4, v4, v188\n"
    "v_exp_f16 v219, v211\n"
    "v_mul_f32 v6, v6, v190\n"
    "v_exp_f16 v220, v212\n"
    "s_waitcnt lgkmcnt(1)\n"
    "v_mul_f32 v8, v8, v192\n"
    "v_exp_f16 v221, v213\n"
    "v_mul_f32 v10, v10, v194\n"
    "v_exp_f16 v222, v214\n"
    "s_waitcnt lgkmcnt(0)\n"
    "v_mul_f32 v12, v12, v196\n"
    "v_exp_f16 v223, v215\n"
    "v_mul_f32 v14, v14, v198\n"
    "v_exp_f16_sdwa v216, v208 dst_sel:WORD_1 dst_unused:UNUSED_PRESERVE src0_sel:WORD_1\n"
    "v_mul_f32 v1, v1, v185\n"
    "v_exp_f16_sdwa v217, v209 dst_sel:WORD_1 dst_unused:UNUSED_PRESERVE src0_sel:WORD_1\n"
    "v_mul_f32 v3, v3, v187\n"
    "v_exp_f16_sdwa v218, v210 dst_sel:WORD_1 dst_unused:UNUSED_PRESERVE src0_sel:WORD_1\n"
    "v_mul_f32 v5, v5, v189\n"
    "v_exp_f16_sdwa v219, v211 dst_sel:WORD_1 dst_unused:UNUSED_PRESERVE src0_sel:WORD_1\n"
    "v_mul_f32 v7, v7, v191\n"
    "v_exp_f16_sdwa v220, v212 dst_sel:WORD_1 dst_unused:UNUSED_PRESERVE src0_sel:WORD_1\n"
    "v_mul_f32 v9, v9, v193\n"
    "v_exp_f16_sdwa v221, v213 dst_sel:WORD_1 dst_unused:UNUSED_PRESERVE src0_sel:WORD_1\n"
    "v_mul_f32 v11, v11, v195\n"
    "v_exp_f16_sdwa v222, v214 dst_sel:WORD_1 dst_unused:UNUSED_PRESERVE src0_sel:WORD_1\n"
    "v_mul_f32 v13, v13, v197\n"
    "v_exp_f16_sdwa v223, v215 dst_sel:WORD_1 dst_unused:UNUSED_PRESERVE src0_sel:WORD_1\n"
    "v_mul_f32 v15, v15, v199\n"
    "v_pk_add_f16 v216, v216, 1.0 op_sel_hi:[1,0]\n"
    "v_pk_add_f16 v217, v217, 1.0 op_sel_hi:[1,0]\n"
    "v_pk_add_f16 v218, v218, 1.0 op_sel_hi:[1,0]\n"
    "v_pk_add_f16 v219, v219, 1.0 op_sel_hi:[1,0]\n"
    "v_pk_add_f16 v220, v220, 1.0 op_sel_hi:[1,0]\n"
    "v_pk_add_f16 v221, v221, 1.0 op_sel_hi:[1,0]\n"
    "v_pk_add_f16 v222, v222, 1.0 op_sel_hi:[1,0]\n"
    "v_pk_add_f16 v223, v223, 1.0 op_sel_hi:[1,0]\n"
    "v_rcp_f16 v208, v216\n"
    "v_rcp_f16 v209, v217\n"
    "v_rcp_f16 v210, v218\n"
    "v_rcp_f16 v211, v219\n"
    "v_rcp_f16 v212, v220\n"
    "v_rcp_f16 v213, v221\n"
    "v_rcp_f16 v214, v222\n"
    "v_rcp_f16 v215, v223\n"
    "v_rcp_f16_sdwa v208, v216 dst_sel:WORD_1 dst_unused:UNUSED_PRESERVE src0_sel:WORD_1\n"
    "v_rcp_f16_sdwa v209, v217 dst_sel:WORD_1 dst_unused:UNUSED_PRESERVE src0_sel:WORD_1\n"
    "v_rcp_f16_sdwa v210, v218 dst_sel:WORD_1 dst_unused:UNUSED_PRESERVE src0_sel:WORD_1\n"
    "v_rcp_f16_sdwa v211, v219 dst_sel:WORD_1 dst_unused:UNUSED_PRESERVE src0_sel:WORD_1\n"
    "v_rcp_f16_sdwa v212, v220 dst_sel:WORD_1 dst_unused:UNUSED_PRESERVE src0_sel:WORD_1\n"
    "v_rcp_f16_sdwa v213, v221 dst_sel:WORD_1 dst_unused:UNUSED_PRESERVE src0_sel:WORD_1\n"
    "v_rcp_f16_sdwa v214, v222 dst_sel:WORD_1 dst_unused:UNUSED_PRESERVE src0_sel:WORD_1\n"
    "v_rcp_f16_sdwa v215, v223 dst_sel:WORD_1 dst_unused:UNUSED_PRESERVE src0_sel:WORD_1\n"
    "v_fma_mix_f32 v179, v0, v208, v179 op_sel_hi:[0,1,0]\n"
    "v_fma_mix_f32 v179, v2, v209, v179 op_sel_hi:[0,1,0]\n"
    "v_fma_mix_f32 v179, v4, v210, v179 op_sel_hi:[0,1,0]\n"
    "v_fma_mix_f32 v179, v6, v211, v179 op_sel_hi:[0,1,0]\n"
    "v_fma_mix_f32 v179, v8, v212, v179 op_sel_hi:[0,1,0]\n"
    "v_fma_mix_f32 v179, v10, v213, v179 op_sel_hi:[0,1,0]\n"
    "v_fma_mix_f32 v179, v12, v214, v179 op_sel_hi:[0,1,0]\n"
    "v_fma_mix_f32 v179, v14, v215, v179 op_sel_hi:[0,1,0]\n"
    "v_fma_mix_f32 v179, v1, v208, v179 op_sel:[0,1,0] op_sel_hi:[0,1,0]\n"
    "v_fma_mix_f32 v179, v3, v209, v179 op_sel:[0,1,0] op_sel_hi:[0,1,0]\n"
    "v_fma_mix_f32 v179, v5, v210, v179 op_sel:[0,1,0] op_sel_hi:[0,1,0]\n"
    "v_fma_mix_f32 v179, v7, v211, v179 op_sel:[0,1,0] op_sel_hi:[0,1,0]\n"
    "v_fma_mix_f32 v179, v9, v212, v179 op_sel:[0,1,0] op_sel_hi:[0,1,0]\n"
    "v_fma_mix_f32 v179, v11, v213, v179 op_sel:[0,1,0] op_sel_hi:[0,1,0]\n"
    "v_fma_mix_f32 v179, v13, v214, v179 op_sel:[0,1,0] op_sel_hi:[0,1,0]\n"
    "v_fma_mix_f32 v179, v15, v215, v179 op_sel:[0,1,0] op_sel_hi:[0,1,0]\n"
    "v_mov_b32 v180, 0\n"
    "v_cvt_pk_f16_f32 v208, v16, v17\n"
    "v_cvt_pk_f16_f32 v209, v18, v19\n"
    "v_cvt_pk_f16_f32 v210, v20, v21\n"
    "v_cvt_pk_f16_f32 v211, v22, v23\n"
    "v_cvt_pk_f16_f32 v212, v24, v25\n"
    "v_cvt_pk_f16_f32 v213, v26, v27\n"
    "v_cvt_pk_f16_f32 v214, v28, v29\n"
    "v_cvt_pk_f16_f32 v215, v30, v31\n"
    "v_pk_mul_f16 v216, v208, v208\n"
    "v_pk_mul_f16 v217, v209, v209\n"
    "v_pk_mul_f16 v218, v210, v210\n"
    "v_pk_mul_f16 v219, v211, v211\n"
    "v_pk_mul_f16 v220, v212, v212\n"
    "v_pk_mul_f16 v221, v213, v213\n"
    "v_pk_mul_f16 v222, v214, v214\n"
    "v_pk_mul_f16 v223, v215, v215\n"
    "v_pk_fma_f16 v216, v216, v206, v207\n"
    "v_pk_fma_f16 v217, v217, v206, v207\n"
    "v_pk_fma_f16 v218, v218, v206, v207\n"
    "v_pk_fma_f16 v219, v219, v206, v207\n"
    "v_pk_fma_f16 v220, v220, v206, v207\n"
    "v_pk_fma_f16 v221, v221, v206, v207\n"
    "v_pk_fma_f16 v222, v222, v206, v207\n"
    "v_pk_fma_f16 v223, v223, v206, v207\n"
    "v_pk_mul_f16 v208, v208, v216\n"
    "v_pk_mul_f16 v209, v209, v217\n"
    "v_pk_mul_f16 v210, v210, v218\n"
    "v_pk_mul_f16 v211, v211, v219\n"
    "v_pk_mul_f16 v212, v212, v220\n"
    "v_pk_mul_f16 v213, v213, v221\n"
    "v_pk_mul_f16 v214, v214, v222\n"
    "v_pk_mul_f16 v215, v215, v223\n"
    "v_exp_f16 v216, v208\n"
    "v_mul_f32 v16, v16, v184\n"
    "v_exp_f16 v217, v209\n"
    "v_mul_f32 v18, v18, v186\n"
    "v_exp_f16 v218, v210\n"
    "v_mul_f32 v20, v20, v188\n"
    "v_exp_f16 v219, v211\n"
    "v_mul_f32 v22, v22, v190\n"
    "v_exp_f16 v220, v212\n"
    "v_mul_f32 v24, v24, v192\n"
    "v_exp_f16 v221, v213\n"
    "v_mul_f32 v26, v26, v194\n"
    "v_exp_f16 v222, v214\n"
    "v_mul_f32 v28, v28, v196\n"
    "v_exp_f16 v223, v215\n"
    "v_mul_f32 v30, v30, v198\n"
    "v_exp_f16_sdwa v216, v208 dst_sel:WORD_1 dst_unused:UNUSED_PRESERVE src0_sel:WORD_1\n"
    "v_mul_f32 v17, v17, v185\n"
    "v_exp_f16_sdwa v217, v209 dst_sel:WORD_1 dst_unused:UNUSED_PRESERVE src0_sel:WORD_1\n"
    "v_mul_f32 v19, v19, v187\n"
    "v_exp_f16_sdwa v218, v210 dst_sel:WORD_1 dst_unused:UNUSED_PRESERVE src0_sel:WORD_1\n"
    "v_mul_f32 v21, v21, v189\n"
    "v_exp_f16_sdwa v219, v211 dst_sel:WORD_1 dst_unused:UNUSED_PRESERVE src0_sel:WORD_1\n"
    "v_mul_f32 v23, v23, v191\n"
    "v_exp_f16_sdwa v220, v212 dst_sel:WORD_1 dst_unused:UNUSED_PRESERVE src0_sel:WORD_1\n"
    "v_mul_f32 v25, v25, v193\n"
    "v_exp_f16_sdwa v221, v213 dst_sel:WORD_1 dst_unused:UNUSED_PRESERVE src0_sel:WORD_1\n"
    "v_mul_f32 v27, v27, v195\n"
    "v_exp_f16_sdwa v222, v214 dst_sel:WORD_1 dst_unused:UNUSED_PRESERVE src0_sel:WORD_1\n"
    "v_mul_f32 v29, v29, v197\n"
    "v_exp_f16_sdwa v223, v215 dst_sel:WORD_1 dst_unused:UNUSED_PRESERVE src0_sel:WORD_1\n"
    "v_mul_f32 v31, v31, v199\n"
    "v_pk_add_f16 v216, v216, 1.0 op_sel_hi:[1,0]\n"
    "v_pk_add_f16 v217, v217, 1.0 op_sel_hi:[1,0]\n"
    "v_pk_add_f16 v218, v218, 1.0 op_sel_hi:[1,0]\n"
    "v_pk_add_f16 v219, v219, 1.0 op_sel_hi:[1,0]\n"
    "v_pk_add_f16 v220, v220, 1.0 op_sel_hi:[1,0]\n"
    "v_pk_add_f16 v221, v221, 1.0 op_sel_hi:[1,0]\n"
    "v_pk_add_f16 v222, v222, 1.0 op_sel_hi:[1,0]\n"
    "v_pk_add_f16 v223, v223, 1.0 op_sel_hi:[1,0]\n"
    "v_rcp_f16 v208, v216\n"
    "v_rcp_f16 v209, v217\n"
    "v_rcp_f16 v210, v218\n"
    "v_rcp_f16 v211, v219\n"
    "v_rcp_f16 v212, v220\n"
    "v_rcp_f16 v213, v221\n"
    "v_rcp_f16 v214, v222\n"
    "v_rcp_f16 v215, v223\n"
    "v_rcp_f16_sdwa v208, v216 dst_sel:WORD_1 dst_unused:UNUSED_PRESERVE src0_sel:WORD_1\n"
    "v_rcp_f16_sdwa v209, v217 dst_sel:WORD_1 dst_unused:UNUSED_PRESERVE src0_sel:WORD_1\n"
    "v_rcp_f16_sdwa v210, v218 dst_sel:WORD_1 dst_unused:UNUSED_PRESERVE src0_sel:WORD_1\n"
    "v_rcp_f16_sdwa v211, v219 dst_sel:WORD_1 dst_unused:UNUSED_PRESERVE src0_sel:WORD_1\n"
    "v_rcp_f16_sdwa v212, v220 dst_sel:WORD_1 dst_unused:UNUSED_PRESERVE src0_sel:WORD_1\n"
    "v_rcp_f16_sdwa v213, v221 dst_sel:WORD_1 dst_unused:UNUSED_PRESERVE src0_sel:WORD_1\n"
    "v_rcp_f16_sdwa v214, v222 dst_sel:WORD_1 dst_unused:UNUSED_PRESERVE src0_sel:WORD_1\n"
    "v_rcp_f16_sdwa v215, v223 dst_sel:WORD_1 dst_unused:UNUSED_PRESERVE src0_sel:WORD_1\n"
    "v_fma_mix_f32 v180, v16, v208, v180 op_sel_hi:[0,1,0]\n"
    "v_fma_mix_f32 v180, v18, v209, v180 op_sel_hi:[0,1,0]\n"
    "v_fma_mix_f32 v180, v20, v210, v180 op_sel_hi:[0,1,0]\n"
    "v_fma_mix_f32 v180, v22, v211, v180 op_sel_hi:[0,1,0]\n"
    "v_fma_mix_f32 v180, v24, v212, v180 op_sel_hi:[0,1,0]\n"
    "v_fma_mix_f32 v180, v26, v213, v180 op_sel_hi:[0,1,0]\n"
    "v_fma_mix_f32 v180, v28, v214, v180 op_sel_hi:[0,1,0]\n"
    "v_fma_mix_f32 v180, v30, v215, v180 op_sel_hi:[0,1,0]\n"
    "v_fma_mix_f32 v180, v17, v208, v180 op_sel:[0,1,0] op_sel_hi:[0,1,0]\n"
    "v_fma_mix_f32 v180, v19, v209, v180 op_sel:[0,1,0] op_sel_hi:[0,1,0]\n"
    "v_fma_mix_f32 v180, v21, v210, v180 op_sel:[0,1,0] op_sel_hi:[0,1,0]\n"
    "v_fma_mix_f32 v180, v23, v211, v180 op_sel:[0,1,0] op_sel_hi:[0,1,0]\n"
    "v_fma_mix_f32 v180, v25, v212, v180 op_sel:[0,1,0] op_sel_hi:[0,1,0]\n"
    "v_fma_mix_f32 v180, v27, v213, v180 op_sel:[0,1,0] op_sel_hi:[0,1,0]\n"
    "v_fma_mix_f32 v180, v29, v214, v180 op_sel:[0,1,0] op_sel_hi:[0,1,0]\n"
    "v_fma_mix_f32 v180, v31, v215, v180 op_sel:[0,1,0] op_sel_hi:[0,1,0]\n"
    "ds_read_b128 v[184:187], v171 offset:2176\n"
    "ds_read_b128 v[188:191], v171 offset:2208\n"
    "ds_read_b128 v[192:195], v171 offset:2240\n"
    "ds_read_b128 v[196:199], v171 offset:2272\n"
    "v_cvt_pk_f16_f32 v208, v32, v33\n"
    "v_cvt_pk_f16_f32 v209, v34, v35\n"
    "v_cvt_pk_f16_f32 v210, v36, v37\n"
    "v_cvt_pk_f16_f32 v211, v38, v39\n"
    "v_cvt_pk_f16_f32 v212, v40, v41\n"
    "v_cvt_pk_f16_f32 v213, v42, v43\n"
    "v_cvt_pk_f16_f32 v214, v44, v45\n"
    "v_cvt_pk_f16_f32 v215, v46, v47\n"
    "v_pk_mul_f16 v216, v208, v208\n"
    "v_pk_mul_f16 v217, v209, v209\n"
    "v_pk_mul_f16 v218, v210, v210\n"
    "v_pk_mul_f16 v219, v211, v211\n"
    "v_pk_mul_f16 v220, v212, v212\n"
    "v_pk_mul_f16 v221, v213, v213\n"
    "v_pk_mul_f16 v222, v214, v214\n"
    "v_pk_mul_f16 v223, v215, v215\n"
    "v_pk_fma_f16 v216, v216, v206, v207\n"
    "v_pk_fma_f16 v217, v217, v206, v207\n"
    "v_pk_fma_f16 v218, v218, v206, v207\n"
    "v_pk_fma_f16 v219, v219, v206, v207\n"
    "v_pk_fma_f16 v220, v220, v206, v207\n"
    "v_pk_fma_f16 v221, v221, v206, v207\n"
    "v_pk_fma_f16 v222, v222, v206, v207\n"
    "v_pk_fma_f16 v223, v223, v206, v207\n"
    "v_pk_mul_f16 v208, v208, v216\n"
    "v_pk_mul_f16 v209, v209, v217\n"
    "v_pk_mul_f16 v210, v210, v218\n"
    "v_pk_mul_f16 v211, v211, v219\n"
    "v_pk_mul_f16 v212, v212, v220\n"
    "v_pk_mul_f16 v213, v213, v221\n"
    "v_pk_mul_f16 v214, v214, v222\n"
    "v_pk_mul_f16 v215, v215, v223\n"
    "v_exp_f16 v216, v208\n"
    "s_waitcnt lgkmcnt(3)\n"
    "v_mul_f32 v32, v32, v184\n"
    "v_exp_f16 v217, v209\n"
    "v_mul_f32 v34, v34, v186\n"
    "v_exp_f16 v218, v210\n"
    "s_waitcnt lgkmcnt(2)\n"
    "v_mul_f32 v36, v36, v188\n"
    "v_exp_f16 v219, v211\n"
    "v_mul_f32 v38, v38, v190\n"
    "v_exp_f16 v220, v212\n"
    "s_waitcnt lgkmcnt(1)\n"
    "v_mul_f32 v40, v40, v192\n"
    "v_exp_f16 v221, v213\n"
    "v_mul_f32 v42, v42, v194\n"
    "v_exp_f16 v222, v214\n"
    "s_waitcnt lgkmcnt(0)\n"
    "v_mul_f32 v44, v44, v196\n"
    "v_exp_f16 v223, v215\n"
    "v_mul_f32 v46, v46, v198\n"
    "v_exp_f16_sdwa v216, v208 dst_sel:WORD_1 dst_unused:UNUSED_PRESERVE src0_sel:WORD_1\n"
    "v_mul_f32 v33, v33, v185\n"
    "v_exp_f16_sdwa v217, v209 dst_sel:WORD_1 dst_unused:UNUSED_PRESERVE src0_sel:WORD_1\n"
    "v_mul_f32 v35, v35, v187\n"
    "v_exp_f16_sdwa v218, v210 dst_sel:WORD_1 dst_unused:UNUSED_PRESERVE src0_sel:WORD_1\n"
    "v_mul_f32 v37, v37, v189\n"
    "v_exp_f16_sdwa v219, v211 dst_sel:WORD_1 dst_unused:UNUSED_PRESERVE src0_sel:WORD_1\n"
    "v_mul_f32 v39, v39, v191\n"
    "v_exp_f16_sdwa v220, v212 dst_sel:WORD_1 dst_unused:UNUSED_PRESERVE src0_sel:WORD_1\n"
    "v_mul_f32 v41, v41, v193\n"
    "v_exp_f16_sdwa v221, v213 dst_sel:WORD_1 dst_unused:UNUSED_PRESERVE src0_sel:WORD_1\n"
    "v_mul_f32 v43, v43, v195\n"
    "v_exp_f16_sdwa v222, v214 dst_sel:WORD_1 dst_unused:UNUSED_PRESERVE src0_sel:WORD_1\n"
    "v_mul_f32 v45, v45, v197\n"
    "v_exp_f16_sdwa v223, v215 dst_sel:WORD_1 dst_unused:UNUSED_PRESERVE src0_sel:WORD_1\n"
    "v_mul_f32 v47, v47, v199\n"
    "v_pk_add_f16 v216, v216, 1.0 op_sel_hi:[1,0]\n"
    "v_pk_add_f16 v217, v217, 1.0 op_sel_hi:[1,0]\n"
    "v_pk_add_f16 v218, v218, 1.0 op_sel_hi:[1,0]\n"
    "v_pk_add_f16 v219, v219, 1.0 op_sel_hi:[1,0]\n"
    "v_pk_add_f16 v220, v220, 1.0 op_sel_hi:[1,0]\n"
    "v_pk_add_f16 v221, v221, 1.0 op_sel_hi:[1,0]\n"
    "v_pk_add_f16 v222, v222, 1.0 op_sel_hi:[1,0]\n"
    "v_pk_add_f16 v223, v223, 1.0 op_sel_hi:[1,0]\n"
    "v_rcp_f16 v208, v216\n"
    "v_rcp_f16 v209, v217\n"
    "v_rcp_f16 v210, v218\n"
    "v_rcp_f16 v211, v219\n"
    "v_rcp_f16 v212, v220\n"
    "v_rcp_f16 v213, v221\n"
    "v_rcp_f16 v214, v222\n"
    "v_rcp_f16 v215, v223\n"
    "v_rcp_f16_sdwa v208, v216 dst_sel:WORD_1 dst_unused:UNUSED_PRESERVE src0_sel:WORD_1\n"
    "v_rcp_f16_sdwa v209, v217 dst_sel:WORD_1 dst_unused:UNUSED_PRESERVE src0_sel:WORD_1\n"
    "v_rcp_f16_sdwa v210, v218 dst_sel:WORD_1 dst_unused:UNUSED_PRESERVE src0_sel:WORD_1\n"
    "v_rcp_f16_sdwa v211, v219 dst_sel:WORD_1 dst_unused:UNUSED_PRESERVE src0_sel:WORD_1\n"
    "v_rcp_f16_sdwa v212, v220 dst_sel:WORD_1 dst_unused:UNUSED_PRESERVE src0_sel:WORD_1\n"
    "v_rcp_f16_sdwa v213, v221 dst_sel:WORD_1 dst_unused:UNUSED_PRESERVE src0_sel:WORD_1\n"
    "v_rcp_f16_sdwa v214, v222 dst_sel:WORD_1 dst_unused:UNUSED_PRESERVE src0_sel:WORD_1\n"
    "v_rcp_f16_sdwa v215, v223 dst_sel:WORD_1 dst_unused:UNUSED_PRESERVE src0_sel:WORD_1\n"
    "v_fma_mix_f32 v179, v32, v208, v179 op_sel_hi:[0,1,0]\n"
    "v_fma_mix_f32 v179, v34, v209, v179 op_sel_hi:[0,1,0]\n"
    "v_fma_mix_f32 v179, v36, v210, v179 op_sel_hi:[0,1,0]\n"
    "v_fma_mix_f32 v179, v38, v211, v179 op_sel_hi:[0,1,0]\n"
    "v_fma_mix_f32 v179, v40, v212, v179 op_sel_hi:[0,1,0]\n"
    "v_fma_mix_f32 v179, v42, v213, v179 op_sel_hi:[0,1,0]\n"
    "v_fma_mix_f32 v179, v44, v214, v179 op_sel_hi:[0,1,0]\n"
    "v_fma_mix_f32 v179, v46, v215, v179 op_sel_hi:[0,1,0]\n"
    "v_fma_mix_f32 v179, v33, v208, v179 op_sel:[0,1,0] op_sel_hi:[0,1,0]\n"
    "v_fma_mix_f32 v179, v35, v209, v179 op_sel:[0,1,0] op_sel_hi:[0,1,0]\n"
    "v_fma_mix_f32 v179, v37, v210, v179 op_sel:[0,1,0] op_sel_hi:[0,1,0]\n"
    "v_fma_mix_f32 v179, v39, v211, v179 op_sel:[0,1,0] op_sel_hi:[0,1,0]\n"
    "v_fma_mix_f32 v179, v41, v212, v179 op_sel:[0,1,0] op_sel_hi:[0,1,0]\n"
    "v_fma_mix_f32 v179, v43, v213, v179 op_sel:[0,1,0] op_sel_hi:[0,1,0]\n"
    "v_fma_mix_f32 v179, v45, v214, v179 op_sel:[0,1,0] op_sel_hi:[0,1,0]\n"
    "v_fma_mix_f32 v179, v47, v215, v179 op_sel:[0,1,0] op_sel_hi:[0,1,0]\n"
    "v_cvt_pk_f16_f32 v208, v48, v49\n"
    "v_cvt_pk_f16_f32 v209, v50, v51\n"
    "v_cvt_pk_f16_f32 v210, v52, v53\n"
    "v_cvt_pk_f16_f32 v211, v54, v55\n"
    "v_cvt_pk_f16_f32 v212, v56, v57\n"
    "v_cvt_pk_f16_f32 v213, v58, v59\n"
    "v_cvt_pk_f16_f32 v214, v60, v61\n"
    "v_cvt_pk_f16_f32 v215, v62, v63\n"
    "v_pk_mul_f16 v216, v208, v208\n"
    "v_pk_mul_f16 v217, v209, v209\n"
    "v_pk_mul_f16 v218, v210, v210\n"
    "v_pk_mul_f16 v219, v211, v211\n"
    "v_pk_mul_f16 v220, v212, v212\n"
    "v_pk_mul_f16 v221, v213, v213\n"
    "v_pk_mul_f16 v222, v214, v214\n"
    "v_pk_mul_f16 v223, v215, v215\n"
    "v_pk_fma_f16 v216, v216, v206, v207\n"
    "v_pk_fma_f16 v217, v217, v206, v207\n"
    "v_pk_fma_f16 v218, v218, v206, v207\n"
    "v_pk_fma_f16 v219, v219, v206, v207\n"
    "v_pk_fma_f16 v220, v220, v206, v207\n"
    "v_pk_fma_f16 v221, v221, v206, v207\n"
    "v_pk_fma_f16 v222, v222, v206, v207\n"
    "v_pk_fma_f16 v223, v223, v206, v207\n"
    "v_pk_mul_f16 v208, v208, v216\n"
    "v_pk_mul_f16 v209, v209, v217\n"
    "v_pk_mul_f16 v210, v210, v218\n"
    "v_pk_mul_f16 v211, v211, v219\n"
    "v_pk_mul_f16 v212, v212, v220\n"
    "v_pk_mul_f16 v213, v213, v221\n"
    "v_pk_mul_f16 v214, v214, v222\n"
    "v_pk_mul_f16 v215, v215, v223\n"
    "v_exp_f16 v216, v208\n"
    "v_mul_f32 v48, v48, v184\n"
    "v_exp_f16 v217, v209\n"
    "v_mul_f32 v50, v50, v186\n"
    "v_exp_f16 v218, v210\n"
    "v_mul_f32 v52, v52, v188\n"
    "v_exp_f16 v219, v211\n"
    "v_mul_f32 v54, v54, v190\n"
    "v_exp_f16 v220, v212\n"
    "v_mul_f32 v56, v56, v192\n"
    "v_exp_f16 v221, v213\n"
    "v_mul_f32 v58, v58, v194\n"
    "v_exp_f16 v222, v214\n"
    "v_mul_f32 v60, v60, v196\n"
    "v_exp_f16 v223, v215\n"
    "v_mul_f32 v62, v62, v198\n"
    "v_exp_f16_sdwa v216, v208 dst_sel:WORD_1 dst_unused:UNUSED_PRESERVE src0_sel:WORD_1\n"
    "v_mul_f32 v49, v49, v185\n"
    "v_exp_f16_sdwa v217, v209 dst_sel:WORD_1 dst_unused:UNUSED_PRESERVE src0_sel:WORD_1\n"
    "v_mul_f32 v51, v51, v187\n"
    "v_exp_f16_sdwa v218, v210 dst_sel:WORD_1 dst_unused:UNUSED_PRESERVE src0_sel:WORD_1\n"
    "v_mul_f32 v53, v53, v189\n"
    "v_exp_f16_sdwa v219, v211 dst_sel:WORD_1 dst_unused:UNUSED_PRESERVE src0_sel:WORD_1\n"
    "v_mul_f32 v55, v55, v191\n"
    "v_exp_f16_sdwa v220, v212 dst_sel:WORD_1 dst_unused:UNUSED_PRESERVE src0_sel:WORD_1\n"
    "v_mul_f32 v57, v57, v193\n"
    "v_exp_f16_sdwa v221, v213 dst_sel:WORD_1 dst_unused:UNUSED_PRESERVE src0_sel:WORD_1\n"
    "v_mul_f32 v59, v59, v195\n"
    "v_exp_f16_sdwa v222, v214 dst_sel:WORD_1 dst_unused:UNUSED_PRESERVE src0_sel:WORD_1\n"
    "v_mul_f32 v61, v61, v197\n"
    "v_exp_f16_sdwa v223, v215 dst_sel:WORD_1 dst_unused:UNUSED_PRESERVE src0_sel:WORD_1\n"
    "v_mul_f32 v63, v63, v199\n"
    "v_pk_add_f16 v216, v216, 1.0 op_sel_hi:[1,0]\n"
    "v_pk_add_f16 v217, v217, 1.0 op_sel_hi:[1,0]\n"
    "v_pk_add_f16 v218, v218, 1.0 op_sel_hi:[1,0]\n"
    "v_pk_add_f16 v219, v219, 1.0 op_sel_hi:[1,0]\n"
    "v_pk_add_f16 v220, v220, 1.0 op_sel_hi:[1,0]\n"
    "v_pk_add_f16 v221, v221, 1.0 op_sel_hi:[1,0]\n"
    "v_pk_add_f16 v222, v222, 1.0 op_sel_hi:[1,0]\n"
    "v_pk_add_f16 v223, v223, 1.0 op_sel_hi:[1,0]\n"
    "v_rcp_f16 v208, v216\n"
    "v_rcp_f16 v209, v217\n"
    "v_rcp_f16 v210, v218\n"
    "v_rcp_f16 v211, v219\n"
    "v_rcp_f16 v212, v220\n"
    "v_rcp_f16 v213, v221\n"
    "v_rcp_f16 v214, v222\n"
    "v_rcp_f16 v215, v223\n"
    "v_rcp_f16_sdwa v208, v216 dst_sel:WORD_1 dst_unused:UNUSED_PRESERVE src0_sel:WORD_1\n"
    "v_rcp_f16_sdwa v209, v217 dst_sel:WORD_1 dst_unused:UNUSED_PRESERVE src0_sel:WORD_1\n"
    "v_rcp_f16_sdwa v210, v218 dst_sel:WORD_1 dst_unused:UNUSED_PRESERVE src0_sel:WORD_1\n"
    "v_rcp_f16_sdwa v211, v219 dst_sel:WORD_1 dst_unused:UNUSED_PRESERVE src0_sel:WORD_1\n"
    "v_rcp_f16_sdwa v212, v220 dst_sel:WORD_1 dst_unused:UNUSED_PRESERVE src0_sel:WORD_1\n"
    "v_rcp_f16_sdwa v213, v221 dst_sel:WORD_1 dst_unused:UNUSED_PRESERVE src0_sel:WORD_1\n"
    "v_rcp_f16_sdwa v214, v222 dst_sel:WORD_1 dst_unused:UNUSED_PRESERVE src0_sel:WORD_1\n"
    "v_rcp_f16_sdwa v215, v223 dst_sel:WORD_1 dst_unused:UNUSED_PRESERVE src0_sel:WORD_1\n"
    "v_fma_mix_f32 v180, v48, v208, v180 op_sel_hi:[0,1,0]\n"
    "v_fma_mix_f32 v180, v50, v209, v180 op_sel_hi:[0,1,0]\n"
    "v_fma_mix_f32 v180, v52, v210, v180 op_sel_hi:[0,1,0]\n"
    "v_fma_mix_f32 v180, v54, v211, v180 op_sel_hi:[0,1,0]\n"
    "v_fma_mix_f32 v180, v56, v212, v180 op_sel_hi:[0,1,0]\n"
    "v_fma_mix_f32 v180, v58, v213, v180 op_sel_hi:[0,1,0]\n"
    "v_fma_mix_f32 v180, v60, v214, v180 op_sel_hi:[0,1,0]\n"
    "v_fma_mix_f32 v180, v62, v215, v180 op_sel_hi:[0,1,0]\n"
    "v_fma_mix_f32 v180, v49, v208, v180 op_sel:[0,1,0] op_sel_hi:[0,1,0]\n"
    "v_fma_mix_f32 v180, v51, v209, v180 op_sel:[0,1,0] op_sel_hi:[0,1,0]\n"
    "v_fma_mix_f32 v180, v53, v210, v180 op_sel:[0,1,0] op_sel_hi:[0,1,0]\n"
    "v_fma_mix_f32 v180, v55, v211, v180 op_sel:[0,1,0] op_sel_hi:[0,1,0]\n"
    "v_fma_mix_f32 v180, v57, v212, v180 op_sel:[0,1,0] op_sel_hi:[0,1,0]\n"
    "v_fma_mix_f32 v180, v59, v213, v180 op_sel:[0,1,0] op_sel_hi:[0,1,0]\n"
    "v_fma_mix_f32 v180, v61, v214, v180 op_sel:[0,1,0] op_sel_hi:[0,1,0]\n"
    "v_fma_mix_f32 v180, v63, v215, v180 op_sel:[0,1,0] op_sel_hi:[0,1,0]\n"
    "ds_read_b128 v[184:187], v171 offset:2304\n"
    "ds_read_b128 v[188:191], v171 offset:2336\n"
    "ds_read_b128 v[192:195], v171 offset:2368\n"
    "ds_read_b128 v[196:199], v171 offset:2400\n"
    "ds_read_b128 v[240:243], v178 offset:44592\n"
    "v_cvt_pk_f16_f32 v208, v64, v65\n"
    "v_cvt_pk_f16_f32 v209, v66, v67\n"
    "v_cvt_pk_f16_f32 v210, v68, v69\n"
    "v_cvt_pk_f16_f32 v211, v70, v71\n"
    "v_cvt_pk_f16_f32 v212, v72, v73\n"
    "v_cvt_pk_f16_f32 v213, v74, v75\n"
    "v_cvt_pk_f16_f32 v214, v76, v77\n"
    "v_cvt_pk_f16_f32 v215, v78, v79\n"
    "v_pk_mul_f16 v216, v208, v208\n"
    "v_pk_mul_f16 v217, v209, v209\n"
    "v_pk_mul_f16 v218, v210, v210\n"
    "v_pk_mul_f16 v219, v211, v211\n"
    "v_pk_mul_f16 v220, v212, v212\n"
    "v_pk_mul_f16 v221, v213, v213\n"
    "v_pk_mul_f16 v222, v214, v214\n"
    "v_pk_mul_f16 v223, v215, v215\n"
    "v_pk_fma_f16 v216, v216, v206, v207\n"
    "v_pk_fma_f16 v217, v217, v206, v207\n"
    "v_pk_fma_f16 v218, v218, v206, v207\n"
    "v_pk_fma_f16 v219, v219, v206, v207\n"
    "v_pk_fma_f16 v220, v220, v206, v207\n"
    "v_pk_fma_f16 v221, v221, v206, v207\n"
    "v_pk_fma_f16 v222, v222, v206, v207\n"
    "v_pk_fma_f16 v223, v223, v206, v207\n"
    "v_pk_mul_f16 v208, v208, v216\n"
    "v_pk_mul_f16 v209, v209, v217\n"
    "v_pk_mul_f16 v210, v210, v218\n"
    "v_pk_mul_f16 v211, v211, v219\n"
    "v_pk_mul_f16 v212, v212, v220\n"
    "v_pk_mul_f16 v213, v213, v221\n"
    "v_pk_mul_f16 v214, v214, v222\n"
    "v_pk_mul_f16 v215, v215, v223\n"
    "v_exp_f16 v216, v208\n"
    "s_waitcnt lgkmcnt(4)\n"
    "v_mul_f32 v64, v64, v184\n"
    "v_exp_f16 v217, v209\n"
    "v_mul_f32 v66, v66, v186\n"
    "v_exp_f16 v218, v210\n"
    "s_waitcnt lgkmcnt(3)\n"
    "v_mul_f32 v68, v68, v188\n"
    "v_exp_f16 v219, v211\n"
    "v_mul_f32 v70, v70, v190\n"
    "v_exp_f16 v220, v212\n"
    "s_waitcnt lgkmcnt(2)\n"
    "v_mul_f32 v72, v72, v192\n"
    "v_exp_f16 v221, v213\n"
    "v_mul_f32 v74, v74, v194\n"
    "v_exp_f16 v222, v214\n"
    "s_waitcnt lgkmcnt(1)\n"
    "v_mul_f32 v76, v76, v196\n"
    "v_exp_f16 v223, v215\n"
    "v_mul_f32 v78, v78, v198\n"
    "v_exp_f16_sdwa v216, v208 dst_sel:WORD_1 dst_unused:UNUSED_PRESERVE src0_sel:WORD_1\n"
    "v_mul_f32 v65, v65, v185\n"
    "v_exp_f16_sdwa v217, v209 dst_sel:WORD_1 dst_unused:UNUSED_PRESERVE src0_sel:WORD_1\n"
    "v_mul_f32 v67, v67, v187\n"
    "v_exp_f16_sdwa v218, v210 dst_sel:WORD_1 dst_unused:UNUSED_PRESERVE src0_sel:WORD_1\n"
    "v_mul_f32 v69, v69, v189\n"
    "v_exp_f16_sdwa v219, v211 dst_sel:WORD_1 dst_unused:UNUSED_PRESERVE src0_sel:WORD_1\n"
    "v_mul_f32 v71, v71, v191\n"
    "v_exp_f16_sdwa v220, v212 dst_sel:WORD_1 dst_unused:UNUSED_PRESERVE src0_sel:WORD_1\n"
    "v_mul_f32 v73, v73, v193\n"
    "v_exp_f16_sdwa v221, v213 dst_sel:WORD_1 dst_unused:UNUSED_PRESERVE src0_sel:WORD_1\n"
    "v_mul_f32 v75, v75, v195\n"
    "v_exp_f16_sdwa v222, v214 dst_sel:WORD_1 dst_unused:UNUSED_PRESERVE src0_sel:WORD_1\n"
    "v_mul_f32 v77, v77, v197\n"
    "v_exp_f16_sdwa v223, v215 dst_sel:WORD_1 dst_unused:UNUSED_PRESERVE src0_sel:WORD_1\n"
    "v_mul_f32 v79, v79, v199\n"
    "v_pk_add_f16 v216, v216, 1.0 op_sel_hi:[1,0]\n"
    "v_pk_add_f16 v217, v217, 1.0 op_sel_hi:[1,0]\n"
    "v_pk_add_f16 v218, v218, 1.0 op_sel_hi:[1,0]\n"
    "v_pk_add_f16 v219, v219, 1.0 op_sel_hi:[1,0]\n"
    "v_pk_add_f16 v220, v220, 1.0 op_sel_hi:[1,0]\n"
    "v_pk_add_f16 v221, v221, 1.0 op_sel_hi:[1,0]\n"
    "v_pk_add_f16 v222, v222, 1.0 op_sel_hi:[1,0]\n"
    "v_pk_add_f16 v223, v223, 1.0 op_sel_hi:[1,0]\n"
    "v_rcp_f16 v208, v216\n"
    "v_rcp_f16 v209, v217\n"
    "v_rcp_f16 v210, v218\n"
    "v_rcp_f16 v211, v219\n"
    "v_rcp_f16 v212, v220\n"
    "v_rcp_f16 v213, v221\n"
    "v_rcp_f16 v214, v222\n"
    "v_rcp_f16 v215, v223\n"
    "v_rcp_f16_sdwa v208, v216 dst_sel:WORD_1 dst_unused:UNUSED_PRESERVE src0_sel:WORD_1\n"
    "v_rcp_f16_sdwa v209, v217 dst_sel:WORD_1 dst_unused:UNUSED_PRESERVE src0_sel:WORD_1\n"
    "v_rcp_f16_sdwa v210, v218 dst_sel:WORD_1 dst_unused:UNUSED_PRESERVE src0_sel:WORD_1\n"
    "v_rcp_f16_sdwa v211, v219 dst_sel:WORD_1 dst_unused:UNUSED_PRESERVE src0_sel:WORD_1\n"
    "v_rcp_f16_sdwa v212, v220 dst_sel:WORD_1 dst_unused:UNUSED_PRESERVE src0_sel:WORD_1\n"
    "v_rcp_f16_sdwa v213, v221 dst_sel:WORD_1 dst_unused:UNUSED_PRESERVE src0_sel:WORD_1\n"
    "v_rcp_f16_sdwa v214, v222 dst_sel:WORD_1 dst_unused:UNUSED_PRESERVE src0_sel:WORD_1\n"
    "v_rcp_f16_sdwa v215, v223 dst_sel:WORD_1 dst_unused:UNUSED_PRESERVE src0_sel:WORD_1\n"
    "v_fma_mix_f32 v179, v64, v208, v179 op_sel_hi:[0,1,0]\n"
    "v_fma_mix_f32 v179, v66, v209, v179 op_sel_hi:[0,1,0]\n"
    "v_fma_mix_f32 v179, v68, v210, v179 op_sel_hi:[0,1,0]\n"
    "v_fma_mix_f32 v179, v70, v211, v179 op_sel_hi:[0,1,0]\n"
    "v_fma_mix_f32 v179, v72, v212, v179 op_sel_hi:[0,1,0]\n"
    "v_fma_mix_f32 v179, v74, v213, v179 op_sel_hi:[0,1,0]\n"
    "v_fma_mix_f32 v179, v76, v214, v179 op_sel_hi:[0,1,0]\n"
    "v_fma_mix_f32 v179, v78, v215, v179 op_sel_hi:[0,1,0]\n"
    "v_fma_mix_f32 v179, v65, v208, v179 op_sel:[0,1,0] op_sel_hi:[0,1,0]\n"
    "v_fma_mix_f32 v179, v67, v209, v179 op_sel:[0,1,0] op_sel_hi:[0,1,0]\n"
    "v_fma_mix_f32 v179, v69, v210, v179 op_sel:[0,1,0] op_sel_hi:[0,1,0]\n"
    "v_fma_mix_f32 v179, v71, v211, v179 op_sel:[0,1,0] op_sel_hi:[0,1,0]\n"
    "v_fma_mix_f32 v179, v73, v212, v179 op_sel:[0,1,0] op_sel_hi:[0,1,0]\n"
    "v_fma_mix_f32 v179, v75, v213, v179 op_sel:[0,1,0] op_sel_hi:[0,1,0]\n"
    "v_fma_mix_f32 v179, v77, v214, v179 op_sel:[0,1,0] op_sel_hi:[0,1,0]\n"
    "v_fma_mix_f32 v179, v79, v215, v179 op_sel:[0,1,0] op_sel_hi:[0,1,0]\n"
    "s_waitcnt lgkmcnt(0)\n"
    "v_lshrrev_b32 v244, 23, v242\n"
    "v_subrev_u32 v240, 0x2800, v240\n"
    "v_subrev_u32 v241, 0x2800, v241\n"
    "v_subrev_u32 v244, 125, v244\n"
    "v_max_u32 v240, v240, v241\n"
    "v_cmp_lt_u32 s[88:89], 8, v244\n"
    "v_cmp_ne_u32 s[90:91], 4, v243\n"
    "v_cmp_le_u32 vcc, 0x5400, v240\n"
    "s_or_b64 s[88:89], s[88:89], s[90:91]\n"
    "s_or_b64 vcc, vcc, s[88:89]\n"
    "s_cbranch_vccnz Lvwait\n"
    "Lvwr:\n"
    "v_cvt_pk_f16_f32 v208, v80, v81\n"
    "v_cvt_pk_f16_f32 v209, v82, v83\n"
    "v_cvt_pk_f16_f32 v210, v84, v85\n"
    "v_cvt_pk_f16_f32 v211, v86, v87\n"
    "v_cvt_pk_f16_f32 v212, v88, v89\n"
    "v_cvt_pk_f16_f32 v213, v90, v91\n"
    "v_cvt_pk_f16_f32 v214, v92, v93\n"
    "v_cvt_pk_f16_f32 v215, v94, v95\n"
    "v_pk_mul_f16 v216, v208, v208\n"
    "v_pk_mul_f16 v217, v209, v209\n"
    "v_pk_mul_f16 v218, v210, v210\n"
    "v_pk_mul_f16 v219, v211, v211\n"
    "v_pk_mul_f16 v220, v212, v212\n"
    "v_pk_mul_f16 v221, v213, v213\n"
    "v_pk_mul_f16 v222, v214, v214\n"
    "v_pk_mul_f16 v223, v215, v215\n"
    "v_pk_fma_f16 v216, v216, v206, v207\n"
    "v_pk_fma_f16 v217, v217, v206, v207\n"
    "v_pk_fma_f16 v218, v218, v206, v207\n"
    "v_pk_fma_f16 v219, v219, v206, v207\n"
    "v_pk_fma_f16 v220, v220, v206, v207\n"
    "v_pk_fma_f16 v221, v221, v206, v207\n"
    "v_pk_fma_f16 v222, v222, v206, v207\n"
    "v_pk_fma_f16 v223, v223, v206, v207\n"
    "v_pk_mul_f16 v208, v208, v216\n"
    "v_pk_mul_f16 v209, v209, v217\n"
    "v_pk_mul_f16 v210, v210, v218\n"
    "v_pk_mul_f16 v211, v211, v219\n"
    "v_pk_mul_f16 v212, v212, v220\n"
    "v_pk_mul_f16 v213, v213, v221\n"
    "v_pk_mul_f16 v214, v214, v222\n"
    "v_pk_mul_f16 v215, v215, v223\n"
    "v_exp_f16 v216, v208\n"
    "v_mul_f32 v80, v80, v184\n"
    "v_exp_f16 v217, v209\n"
    "v_mul_f32 v82, v82, v186\n"
    "v_exp_f16 v218, v210\n"
    "v_mul_f32 v84, v84, v188\n"
    "v_exp_f16 v219, v211\n"
    "v_mul_f32 v86, v86, v190\n"
    "v_exp_f16 v220, v212\n"
    "v_mul_f32 v88, v88, v192\n"
    "v_exp_f16 v221, v213\n"
    "v_mul_f32 v90, v90, v194\n"
    "v_exp_f16 v222, v214\n"
    "v_mul_f32 v92, v92, v196\n"
    "v_exp_f16 v223, v215\n"
    "v_mul_f32 v94, v94, v198\n"
    "v_exp_f16_sdwa v216, v208 dst_sel:WORD_1 dst_unused:UNUSED_PRESERVE src0_sel:WORD_1\n"
    "v_mul_f32 v81, v81, v185\n"
    "v_exp_f16_sdwa v217, v209 dst_sel:WORD_1 dst_unused:UNUSED_PRESERVE src0_sel:WORD_1\n"
    "v_mul_f32 v83, v83, v187\n"
    "v_exp_f16_sdwa v218, v210 dst_sel:WORD_1 dst_unused:UNUSED_PRESERVE src0_sel:WORD_1\n"
    "v_mul_f32 v85, v85, v189\n"
    "v_exp_f16_sdwa v219, v211 dst_sel:WORD_1 dst_unused:UNUSED_PRESERVE src0_sel:WORD_1\n"
    "v_mul_f32 v87, v87, v191\n"
    "v_exp_f16_sdwa v220, v212 dst_sel:WORD_1 dst_unused:UNUSED_PRESERVE src0_sel:WORD_1\n"
    "v_mul_f32 v89, v89, v193\n"
    "v_exp_f16_sdwa v221, v213 dst_sel:WORD_1 dst_unused:UNUSED_PRESERVE src0_sel:WORD_1\n"
    "v_mul_f32 v91, v91, v195\n"
    "v_exp_f16_sdwa v222, v214 dst_sel:WORD_1 dst_unused:UNUSED_PRESERVE src0_sel:WORD_1\n"
    "v_mul_f32 v93, v93, v197\n"
    "v_exp_f16_sdwa v223, v215 dst_sel:WORD_1 dst_unused:UNUSED_PRESERVE src0_sel:WORD_1\n"
    "v_mul_f32 v95, v95, v199\n"
    "v_pk_add_f16 v216, v216, 1.0 op_sel_hi:[1,0]\n"
    "v_pk_add_f16 v217, v217, 1.0 op_sel_hi:[1,0]\n"
    "v_pk_add_f16 v218, v218, 1.0 op_sel_hi:[1,0]\n"
    "v_pk_add_f16 v219, v219, 1.0 op_sel_hi:[1,0]\n"
    "v_pk_add_f16 v220, v220, 1.0 op_sel_hi:[1,0]\n"
    "v_pk_add_f16 v221, v221, 1.0 op_sel_hi:[1,0]\n"
    "v_pk_add_f16 v222, v222, 1.0 op_sel_hi:[1,0]\n"
    "v_pk_add_f16 v223, v223, 1.0 op_sel_hi:[1,0]\n"
    "v_rcp_f16 v208, v216\n"
    "v_rcp_f16 v209, v217\n"
    "v_rcp_f16 v210, v218\n"
    "v_rcp_f16 v211, v219\n"
    "v_rcp_f16 v212, v220\n"
    "v_rcp_f16 v213, v221\n"
    "v_rcp_f16 v214, v222\n"
    "v_rcp_f16 v215, v223\n"
    "v_rcp_f16_sdwa v208, v216 dst_sel:WORD_1 dst_unused:UNUSED_PRESERVE src0_sel:WORD_1\n"
    "v_rcp_f16_sdwa v209, v217 dst_sel:WORD_1 dst_unused:UNUSED_PRESERVE src0_sel:WORD_1\n"
    "v_rcp_f16_sdwa v210, v218 dst_sel:WORD_1 dst_unused:UNUSED_PRESERVE src0_sel:WORD_1\n"
    "v_rcp_f16_sdwa v211, v219 dst_sel:WORD_1 dst_unused:UNUSED_PRESERVE src0_sel:WORD_1\n"
    "v_rcp_f16_sdwa v212, v220 dst_sel:WORD_1 dst_unused:UNUSED_PRESERVE src0_sel:WORD_1\n"
    "v_rcp_f16_sdwa v213, v221 dst_sel:WORD_1 dst_unused:UNUSED_PRESERVE src0_sel:WORD_1\n"
    "v_rcp_f16_sdwa v214, v222 dst_sel:WORD_1 dst_unused:UNUSED_PRESERVE src0_sel:WORD_1\n"
    "v_rcp_f16_sdwa v215, v223 dst_sel:WORD_1 dst_unused:UNUSED_PRESERVE src0_sel:WORD_1\n"
    "v_fma_mix_f32 v180, v80, v208, v180 op_sel_hi:[0,1,0]\n"
    "v_fma_mix_f32 v180, v82, v209, v180 op_sel_hi:[0,1,0]\n"
    "v_fma_mix_f32 v180, v84, v210, v180 op_sel_hi:[0,1,0]\n"
    "v_fma_mix_f32 v180, v86, v211, v180 op_sel_hi:[0,1,0]\n"
    "v_fma_mix_f32 v180, v88, v212, v180 op_sel_hi:[0,1,0]\n"
    "v_fma_mix_f32 v180, v90, v213, v180 op_sel_hi:[0,1,0]\n"
    "v_fma_mix_f32 v180, v92, v214, v180 op_sel_hi:[0,1,0]\n"
    "v_fma_mix_f32 v180, v94, v215, v180 op_sel_hi:[0,1,0]\n"
    "v_fma_mix_f32 v180, v81, v208, v180 op_sel:[0,1,0] op_sel_hi:[0,1,0]\n"
    "v_fma_mix_f32 v180, v83, v209, v180 op_sel:[0,1,0] op_sel_hi:[0,1,0]\n"
    "v_fma_mix_f32 v180, v85, v210, v180 op_sel:[0,1,0] op_sel_hi:[0,1,0]\n"
    "v_fma_mix_f32 v180, v87, v211, v180 op_sel:[0,1,0] op_sel_hi:[0,1,0]\n"
    "v_fma_mix_f32 v180, v89, v212, v180 op_sel:[0,1,0] op_sel_hi:[0,1,0]\n"
    "v_fma_mix_f32 v180, v91, v213, v180 op_sel:[0,1,0] op_sel_hi:[0,1,0]\n"
    "v_fma_mix_f32 v180, v93, v214, v180 op_sel:[0,1,0] op_sel_hi:[0,1,0]\n"
    "v_fma_mix_f32 v180, v95, v215, v180 op_sel:[0,1,0] op_sel_hi:[0,1,0]\n"
    "s_xor_b32 s53, s65, 2\n"
    "s_lshl_b32 s52, s53, 2\n"
    "v_mov_b32 v204, s52\n"
    "s_mul_i32 s53, s53, 7168\n"
    "s_add_u32 s53, s53, 45696\n"
    "v_add_u32 v202, s53, v202\n"
    "s_lshl_b32 s52, s63, 6\n"
    "v_add_u32 v171, s52, v171\n"
    "s_mov_b32 s58, 0x40000\n"
    "s_branch Lspx_in\n"
    "Lspx:\n"
    "s_sleep 1\n"
    "Lspx_in:\n"
    "ds_read_b32 v205, v204 offset:44544\n"
    "s_sub_u32 s58, s58, 1\n"
    "s_cbranch_scc1 Lspx_done\n"
    "s_waitcnt lgkmcnt(0)\n"
    "v_readfirstlane_b32 s52, v205\n"
    "s_nop 0\n"
    "s_cmp_eq_u32 s52, 1\n"
    "s_cbranch_scc0 Lspx\n"
    "Lspx_done:\n"
    "ds_read_b128 v[104:107], v202 offset:1024\n"
    "ds_read_b128 v[108:111], v202 offset:2048\n"
    "ds_read_b128 v[184:187], v171 offset:2432\n"
    "ds_read_b128 v[188:191], v171 offset:2464\n"
    "s_waitcnt lgkmcnt(3)\n"
    "v_add_f32 v96, v96, v104\n"
    "v_add_f32 v97, v97, v105\n"
    "v_add_f32 v98, v98, v106\n"
    "v_add_f32 v99, v99, v107\n"
    "s_waitcnt lgkmcnt(2)\n"
    "v_add_f32 v100, v100, v108\n"
    "v_add_f32 v101, v101, v109\n"
    "v_add_f32 v102, v102, v110\n"
    "v_add_f32 v103, v103, v111\n"
    "v_mov_b32 v181, 0\n"
    "v_cvt_pk_f16_f32 v208, v96, v97\n"
    "v_cvt_pk_f16_f32 v209, v98, v99\n"
    "v_cvt_pk_f16_f32 v210, v100, v101\n"
    "v_cvt_pk_f16_f32 v211, v102, v103\n"
    "v_pk_mul_f16 v216, v208, v208\n"
    "v_pk_mul_f16 v217, v209, v209\n"
    "v_pk_mul_f16 v218, v210, v210\n"
    "v_pk_mul_f16 v219, v211, v211\n"
    "v_pk_fma_f16 v216, v216, v206, v207\n"
    "v_pk_fma_f16 v217, v217, v206, v207\n"
    "v_pk_fma_f16 v218, v218, v206, v207\n"
    "v_pk_fma_f16 v219, v219, v206, v207\n"
    "v_pk_mul_f16 v208, v208, v216\n"
    "v_pk_mul_f16 v209, v209, v217\n"
    "v_pk_mul_f16 v210, v210, v218\n"
    "v_pk_mul_f16 v211, v211, v219\n"
    "v_exp_f16 v216, v208\n"
    "s_waitcnt lgkmcnt(1)\n"
    "v_mul_f32 v96, v96, v184\n"
    "v_exp_f16 v217, v209\n"
    "v_mul_f32 v98, v98, v186\n"
    "v_exp_f16 v218, v210\n"
    "s_waitcnt lgkmcnt(0)\n"
    "v_mul_f32 v100, v100, v188\n"
    "v_exp_f16 v219, v211\n"
    "v_mul_f32 v102, v102, v190\n"
    "v_exp_f16_sdwa v216, v208 dst_sel:WORD_1 dst_unused:UNUSED_PRESERVE src0_sel:WORD_1\n"
    "v_mul_f32 v97, v97, v185\n"
    "v_exp_f16_sdwa v217, v209 dst_sel:WORD_1 dst_unused:UNUSED_PRESERVE src0_sel:WORD_1\n"
    "v_mul_f32 v99, v99, v187\n"
    "v_exp_f16_sdwa v218, v210 dst_sel:WORD_1 dst_unused:UNUSED_PRESERVE src0_sel:WORD_1\n"
    "v_mul_f32 v101, v101, v189\n"
    "v_exp_f16_sdwa v219, v211 dst_sel:WORD_1 dst_unused:UNUSED_PRESERVE src0_sel:WORD_1\n"
    "v_mul_f32 v103, v103, v191\n"
    "v_pk_add_f16 v216, v216, 1.0 op_sel_hi:[1,0]\n"
    "v_pk_add_f16 v217, v217, 1.0 op_sel_hi:[1,0]\n"
    "v_pk_add_f16 v218, v218, 1.0 op_sel_hi:[1,0]\n"
    "v_pk_add_f16 v219, v219, 1.0 op_sel_hi:[1,0]\n"
    "v_rcp_f16 v208, v216\n"
    "v_rcp_f16 v209, v217\n"
    "v_rcp_f16 v210, v218\n"
    "v_rcp_f16 v211, v219\n"
    "v_rcp_f16_sdwa v208, v216 dst_sel:WORD_1 dst_unused:UNUSED_PRESERVE src0_sel:WORD_1\n"
    "v_rcp_f16_sdwa v209, v217 dst_sel:WORD_1 dst_unused:UNUSED_PRESERVE src0_sel:WORD_1\n"
    "v_rcp_f16_sdwa v210, v218 dst_sel:WORD_1 dst_unused:UNUSED_PRESERVE src0_sel:WORD_1\n"
    "v_rcp_f16_sdwa v211, v219 dst_sel:WORD_1 dst_unused:UNUSED_PRESERVE src0_sel:WORD_1\n"
    "v_fma_mix_f32 v181, v96, v208, v181 op_sel_hi:[0,1,0]\n"
    "v_fma_mix_f32 v181, v98, v209, v181 op_sel_hi:[0,1,0]\n"
    "v_fma_mix_f32 v181, v100, v210, v181 op_sel_hi:[0,1,0]\n"
    "v_fma_mix_f32 v181, v102, v211, v181 op_sel_hi:[0,1,0]\n"
    "v_fma_mix_f32 v181, v97, v208, v181 op_sel:[0,1,0] op_sel_hi:[0,1,0]\n"
    "v_fma_mix_f32 v181, v99, v209, v181 op_sel:[0,1,0] op_sel_hi:[0,1,0]\n"
    "v_fma_mix_f32 v181, v101, v210, v181 op_sel:[0,1,0] op_sel_hi:[0,1,0]\n"
    "v_fma_mix_f32 v181, v103, v211, v181 op_sel:[0,1,0] op_sel_hi:[0,1,0]\n"
    "s_waitcnt vmcnt(0)\n"
    "v_lshlrev_b32 v112, 16, v112\n"
    "v_fmac_f32 v179, 0x3e000000, v112\n"
    "v_fmac_f32 v180, 0x3e000000, v112\n"
    "v_fmac_f32 v179, s66, v181\n"
    "v_fmac_f32 v180, s67, v181\n"
    "v_lshlrev_b32 v200, 2, v183\n"
    "v_add_u32 v200, s51, v200\n"
    "ds_write2st64_b32 v200, v179, v180 offset0:0 offset1:1\n"
    "s_waitcnt lgkmcnt(0)\n"
    "s_barrier\n"
    "ds_read_b32 v0, v178 offset:44608\n"
    "ds_read_b32 v1, v178 offset:44624\n"
    "s_waitcnt lgkmcnt(0)\n"
    "v_or_b32 v0, v0, v1\n"
    "s_nop 0\n"
    "v_readfirstlane_b32 s51, v0\n"
    "s_nop 0\n"
    "s_cmp_lg_u32 s51, 0\n"
    "s_cbranch_scc1 Lretry\n"
    "s_endpgm\n"
    "Lretry:\n"
    "s_mov_b64 exec, -1\n"
    "s_setprio 0\n"
    "v_mov_b32 v4, 0\n"
    "s_waitcnt vmcnt(0) lgkmcnt(0)\n"
    "ds_read_b128 v[0:3], v4 offset:44608\n"
    "v_mbcnt_lo_u32_b32 v183, -1, 0\n"
    "v_mbcnt_hi_u32_b32 v183, -1, v183\n"
    "s_add_u32 s47, s47, 1\n"
    "s_waitcnt lgkmcnt(0)\n"
    "v_readfirstlane_b32 s88, v0\n"
    "v_readfirstlane_b32 s89, v1\n"
    "v_readfirstlane_b32 s90, v2\n"
    "v_readfirstlane_b32 s91, v3\n"
    "s_nop 4\n"
    "s_cmp_lg_u32 s88, 0\n"
    "s_cselect_b32 s55, s89, s55\n"
    "s_cselect_b32 s57, s90, s57\n"
    "s_cselect_b32 s46, s91, s46\n"
    "s_barrier\n"
    "s_branch Lagain\n"
    "Lscan:\n"
    "ds_read_b128 v[112:115], v247 offset:0\n"
    "ds_read_b128 v[116:119], v247 offset:1040\n"
    "ds_read_b128 v[120:123], v247 offset:2080\n"
    "ds_read_b128 v[124:127], v247 offset:3120\n"
    "ds_read_b128 v[128:131], v247 offset:4160\n"
    "ds_read_b128 v[132:135], v247 offset:5200\n"
    "ds_read_b128 v[136:139], v247 offset:6240\n"
    "ds_read_b128 v[140:143], v247 offset:7280\n"
    "ds_read_b128 v[144:147], v247 offset:8320\n"
    "ds_read_b128 v[148:151], v247 offset:9360\n"
    "ds_read_b128 v[152:155], v247 offset:10400\n"
    "ds_read_b128 v[156:159], v247 offset:11440\n"
    "s_waitcnt lgkmcnt(0)\n"
    "v_pk_maximum3_f16 v240, v240, v112, v112 neg_lo:[0,0,1] neg_hi:[0,0,1]\n"
    "v_pk_maximum3_f16 v240, v240, v113, v113 neg_lo:[0,0,1] neg_hi:[0,0,1]\n"
    "v_pk_maximum3_f16 v240, v240, v114, v114 neg_lo:[0,0,1] neg_hi:[0,0,1]\n"
    "v_pk_maximum3_f16 v240, v240, v115, v115 neg_lo:[0,0,1] neg_hi:[0,0,1]\n"
    "v_pk_maximum3_f16 v240, v240, v116, v116 neg_lo:[0,0,1] neg_hi:[0,0,1]\n"
    "v_pk_maximum3_f16 v240, v240, v117, v117 neg_lo:[0,0,1] neg_hi:[0,0,1]\n"
    "v_pk_maximum3_f16 v240, v240, v118, v118 neg_lo:[0,0,1] neg_hi:[0,0,1]\n"
    "v_pk_maximum3_f16 v240, v240, v119, v119 neg_lo:[0,0,1] neg_hi:[0,0,1]\n"
    "v_pk_maximum3_f16 v240, v240, v120, v120 neg_lo:[0,0,1] neg_hi:[0,0,1]\n"
    "v_pk_maximum3_f16 v240, v240, v121, v121 neg_lo:[0,0,1] neg_hi:[0,0,1]\n"
    "v_pk_maximum3_f16 v240, v240, v122, v122 neg_lo:[0,0,1] neg_hi:[0,0,1]\n"
    "v_pk_maximum3_f16 v240, v240, v123, v123 neg_lo:[0,0,1] neg_hi:[0,0,1]\n"
    "v_pk_maximum3_f16 v240, v240, v124, v124 neg_lo:[0,0,1] neg_hi:[0,0,1]\n"
    "v_pk_maximum3_f16 v240, v240, v125, v125 neg_lo:[0,0,1] neg_hi:[0,0,1]\n"
    "v_pk_maximum3_f16 v240, v240, v126, v126 neg_lo:[0,0,1] neg_hi:[0,0,1]\n"
    "v_pk_maximum3_f16 v240, v240, v127, v127 neg_lo:[0,0,1] neg_hi:[0,0,1]\n"
    "v_pk_maximum3_f16 v240, v240, v128, v128 neg_lo:[0,0,1] neg_hi:[0,0,1]\n"
    "v_pk_maximum3_f16 v240, v240, v129, v129 neg_lo:[0,0,1] neg_hi:[0,0,1]\n"
    "v_pk_maximum3_f16 v240, v240, v130, v130 neg_lo:[0,0,1] neg_hi:[0,0,1]\n"
    "v_pk_maximum3_f16 v240, v240, v131, v131 neg_lo:[0,0,1] neg_hi:[0,0,1]\n"
    "v_pk_maximum3_f16 v240, v240, v132, v132 neg_lo:[0,0,1] neg_hi:[0,0,1]\n"
    "v_pk_maximum3_f16 v240, v240, v133, v133 neg_lo:[0,0,1] neg_hi:[0,0,1]\n"
    "v_pk_maximum3_f16 v240, v240, v134, v134 neg_lo:[0,0,1] neg_hi:[0,0,1]\n"
    "v_pk_maximum3_f16 v240, v240, v135, v135 neg_lo:[0,0,1] neg_hi:[0,0,1]\n"
    "v_pk_maximum3_f16 v240, v240, v136, v136 neg_lo:[0,0,1] neg_hi:[0,0,1]\n"
    "v_pk_maximum3_f16 v240, v240, v137, v137 neg_lo:[0,0,1] neg_hi:[0,0,1]\n"
    "v_pk_maximum3_f16 v240, v240, v138, v138 neg_lo:[0,0,1] neg_hi:[0,0,1]\n"
    "v_pk_maximum3_f16 v240, v240, v139, v139 neg_lo:[0,0,1] neg_hi:[0,0,1]\n"
    "v_pk_maximum3_f16 v240, v240, v140, v140 neg_lo:[0,0,1] neg_hi:[0,0,1]\n"
    "v_pk_maximum3_f16 v240, v240, v141, v141 neg_lo:[0,0,1] neg_hi:[0,0,1]\n"
    "v_pk_maximum3_f16 v240, v240, v142, v142 neg_lo:[0,0,1] neg_hi:[0,0,1]\n"
    "v_pk_maximum3_f16 v240, v240, v143, v143 neg_lo:[0,0,1] neg_hi:[0,0,1]\n"
    "v_pk_maximum3_f16 v240, v240, v144, v144 neg_lo:[0,0,1] neg_hi:[0,0,1]\n"
    "v_pk_maximum3_f16 v240, v240, v145, v145 neg_lo:[0,0,1] neg_hi:[0,0,1]\n"
    "v_pk_maximum3_f16 v240, v240, v146, v146 neg_lo:[0,0,1] neg_hi:[0,0,1]\n"
    "v_pk_maximum3_f16 v240, v240, v147, v147 neg_lo:[0,0,1] neg_hi:[0,0,1]\n"
    "v_pk_maximum3_f16 v240, v240, v148, v148 neg_lo:[0,0,1] neg_hi:[0,0,1]\n"
    "v_pk_maximum3_f16 v240, v240, v149, v149 neg_lo:[0,0,1] neg_hi:[0,0,1]\n"
    "v_pk_maximum3_f16 v240, v240, v150, v150 neg_lo:[0,0,1] neg_hi:[0,0,1]\n"
    "v_pk_maximum3_f16 v240, v240, v151, v151 neg_lo:[0,0,1] neg_hi:[0,0,1]\n"
    "v_pk_maximum3_f16 v240, v240, v152, v152 neg_lo:[0,0,1] neg_hi:[0,0,1]\n"
    "v_pk_maximum3_f16 v240, v240, v153, v153 neg_lo:[0,0,1] neg_hi:[0,0,1]\n"
    "v_pk_maximum3_f16 v240, v240, v154, v154 neg_lo:[0,0,1] neg_hi:[0,0,1]\n"
    "v_pk_maximum3_f16 v240, v240, v155, v155 neg_lo:[0,0,1] neg_hi:[0,0,1]\n"
    "v_pk_maximum3_f16 v240, v240, v156, v156 neg_lo:[0,0,1] neg_hi:[0,0,1]\n"
    "v_pk_maximum3_f16 v240, v240, v157, v157 neg_lo:[0,0,1] neg_hi:[0,0,1]\n"
    "v_pk_maximum3_f16 v240, v240, v158, v158 neg_lo:[0,0,1] neg_hi:[0,0,1]\n"
    "v_pk_maximum3_f16 v240, v240, v159, v159 neg_lo:[0,0,1] neg_hi:[0,0,1]\n"
    "s_branch Lscanr\n"
    "Lvwait:\n"
    "s_mov_b32 s58, 0x40000\n"
    "Lvw1:\n"
    "ds_read_b128 v[240:243], v178 offset:44592\n"
    "s_waitcnt lgkmcnt(0)\n"
    "v_readfirstlane_b32 s95, v243\n"
    "v_readfirstlane_b32 s88, v240\n"
    "v_readfirstlane_b32 s89, v241\n"
    "v_readfirstlane_b32 s90, v242\n"
    "s_sub_u32 s58, s58, 1\n"
    "s_cbranch_scc1 Lvw_done\n"
    "s_cmp_lg_u32 s95, 4\n"
    "s_cbranch_scc1 Lvw1\n"
    "Lvw_done:\n"
    "s_lshr_b32 s95, s90, 23\n"
    "s_sub_i32 s92, 130, s95\n"
    "s_add_i32 s92, s92, s55\n"
    "s_min_i32 s92, s92, 120\n"
    "s_max_i32 s92, s92, -120\n"
    "s_sub_i32 s95, s95, 125\n"
    "s_cmp_le_u32 s95, 8\n"
    "s_cselect_b32 s92, s55, s92\n"
    "s_cmp_eq_u32 s90, 0\n"
    "s_cselect_b32 s92, s55, s92\n"
    "s_lshr_b32 s89, s89, 10\n"
    "s_sub_i32 s95, 22, s89\n"
    "s_cmp_eq_u32 s89, 0\n"
    "s_cselect_b32 s95, 24, s95\n"
    "s_cmp_ge_u32 s89, 10\n"
    "s_cselect_b32 s95, 0, s95\n"
    "s_cmp_eq_u32 s89, 31\n"
    "s_cselect_b32 s95, -20, s95\n"
    "s_add_i32 s93, s57, s95\n"
    "s_min_i32 s93, s93, 120\n"
    "s_max_i32 s93, s93, -120\n"
    "s_lshr_b32 s88, s88, 10\n"
    "s_sub_i32 s95, 22, s88\n"
    "s_cmp_eq_u32 s88, 0\n"
    "s_cselect_b32 s95, 24, s95\n"
    "s_cmp_ge_u32 s88, 10\n"
    "s_cselect_b32 s95, 0, s95\n"
    "s_cmp_eq_u32 s88, 31\n"
    "s_cselect_b32 s95, -20, s95\n"
    "s_add_i32 s94, s46, s95\n"
    "s_add_i32 s94, s94, s92\n"
    "s_sub_i32 s94, s94, s55\n"
    "s_min_i32 s94, s94, 60\n"
    "s_max_i32 s94, s94, -120\n"
    "s_xor_b32 s91, s92, s55\n"
    "s_xor_b32 s95, s93, s57\n"
    "s_or_b32 s91, s91, s95\n"
    "s_xor_b32 s95, s94, s46\n"
    "s_or_b32 s91, s91, s95\n"
    "s_cmp_ge_u32 s47, 9\n"
    "s_cselect_b32 s91, 0, s91\n"
    "s_cmp_eq_u32 s47, 0\n"
    "s_cselect_b32 s92, s55, s92\n"
    "s_cselect_b32 s93, s57, s93\n"
    "s_cselect_b32 s94, s46, s94\n"
    "v_mov_b32 v240, s91\n"
    "v_mov_b32 v241, s92\n"
    "v_mov_b32 v242, s93\n"
    "v_mov_b32 v243, s94\n"
    "ds_write_b128 v178, v[240:243] offset:44608\n"
    "s_branch Lvwr\n"
    "Lovf:\n"
    "s_cmp_lg_u32 s47, 0\n"
    "s_cbranch_scc1 Lovfr\n"
    "ds_write_b32 v71, v71 offset:16\n"
    "s_waitcnt lgkmcnt(0)\n"
    "s_branch Lovfr\n"
    "Lsx:\n"
    "s_waitcnt vmcnt(0)\n"
    "v_mov_b32 v39, 0\n"
    "v_lshlrev_b32 v38, 16, v8\n"
    "v_and_b32 v8, 0xffff0000, v8\n"
    "v_ldexp_f32 v38, v38, s55\n"
    "v_ldexp_f32 v8, v8, s55\n"
    "v_max3_f32 v39, |v38|, |v8|, v39\n"
    "v_cvt_pk_bf16_f32 v8, v38, v8\n"
    "v_lshlrev_b32 v38, 16, v9\n"
    "v_and_b32 v9, 0xffff0000, v9\n"
    "v_ldexp_f32 v38, v38, s55\n"
    "v_ldexp_f32 v9, v9, s55\n"
    "v_max3_f32 v39, |v38|, |v9|, v39\n"
    "v_cvt_pk_bf16_f32 v9, v38, v9\n"
    "v_lshlrev_b32 v38, 16, v10\n"
    "v_and_b32 v10, 0xffff0000, v10\n"
    "v_ldexp_f32 v38, v38, s55\n"
    "v_ldexp_f32 v10, v10, s55\n"
    "v_max3_f32 v39, |v38|, |v10|, v39\n"
    "v_cvt_pk_bf16_f32 v10, v38, v10\n"
    "v_lshlrev_b32 v38, 16, v11\n"
    "v_and_b32 v11, 0xffff0000, v11\n"
    "v_ldexp_f32 v38, v38, s55\n"
    "v_ldexp_f32 v11, v11, s55\n"
    "v_max3_f32 v39, |v38|, |v11|, v39\n"
    "v_cvt_pk_bf16_f32 v11, v38, v11\n"
    "v_lshlrev_b32 v38, 16, v12\n"
    "v_and_b32 v12, 0xffff0000, v12\n"
    "v_ldexp_f32 v38, v38, s55\n"
    "v_ldexp_f32 v12, v12, s55\n"
    "v_max3_f32 v39, |v38|, |v12|, v39\n"
    "v_cvt_pk_bf16_f32 v12, v38, v12\n"
    "v_lshlrev_b32 v38, 16, v13\n"
    "v_and_b32 v13, 0xffff0000, v13\n"
    "v_ldexp_f32 v38, v38, s55\n"
    "v_ldexp_f32 v13, v13, s55\n"
    "v_max3_f32 v39, |v38|, |v13|, v39\n"
    "v_cvt_pk_bf16_f32 v13, v38, v13\n"
    "v_lshlrev_b32 v38, 16, v14\n"
    "v_and_b32 v14, 0xffff0000, v14\n"
    "v_ldexp_f32 v38, v38, s55\n"
    "v_ldexp_f32 v14, v14, s55\n"
    "v_max3_f32 v39, |v38|, |v14|, v39\n"
    "v_cvt_pk_bf16_f32 v14, v38, v14\n"
    "v_lshlrev_b32 v38, 16, v15\n"
    "v_and_b32 v15, 0xffff0000, v15\n"
    "v_ldexp_f32 v38, v38, s55\n"
    "v_ldexp_f32 v15, v15, s55\n"
    "v_max3_f32 v39, |v38|, |v15|, v39\n"
    "v_cvt_pk_bf16_f32 v15, v38, v15\n"
    "v_lshlrev_b32 v38, 16, v16\n"
    "v_and_b32 v16, 0xffff0000, v16\n"
    "v_ldexp_f32 v38, v38, s55\n"
    "v_ldexp_f32 v16, v16, s55\n"
    "v_max3_f32 v39, |v38|, |v16|, v39\n"
    "v_cvt_pk_bf16_f32 v16, v38, v16\n"
    "v_lshlrev_b32 v38, 16, v17\n"
    "v_and_b32 v17, 0xffff0000, v17\n"
    "v_ldexp_f32 v38, v38, s55\n"
    "v_ldexp_f32 v17, v17, s55\n"
    "v_max3_f32 v39, |v38|, |v17|, v39\n"
    "v_cvt_pk_bf16_f32 v17, v38, v17\n"
    "v_lshlrev_b32 v38, 16, v18\n"
    "v_and_b32 v18, 0xffff0000, v18\n"
    "v_ldexp_f32 v38, v38, s55\n"
    "v_ldexp_f32 v18, v18, s55\n"
    "v_max3_f32 v39, |v38|, |v18|, v39\n"
    "v_cvt_pk_bf16_f32 v18, v38, v18\n"
    "v_lshlrev_b32 v38, 16, v19\n"
    "v_and_b32 v19, 0xffff0000, v19\n"
    "v_ldexp_f32 v38, v38, s55\n"
    "v_ldexp_f32 v19, v19, s55\n"
    "v_max3_f32 v39, |v38|, |v19|, v39\n"
    "v_cvt_pk_bf16_f32 v19, v38, v19\n"
    "v_lshlrev_b32 v38, 16, v20\n"
    "v_and_b32 v20, 0xffff0000, v20\n"
    "v_ldexp_f32 v38, v38, s55\n"
    "v_ldexp_f32 v20, v20, s55\n"
    "v_max3_f32 v39, |v38|, |v20|, v39\n"
    "v_cvt_pk_bf16_f32 v20, v38, v20\n"
    "v_lshlrev_b32 v38, 16, v21\n"
    "v_and_b32 v21, 0xffff0000, v21\n"
    "v_ldexp_f32 v38, v38, s55\n"
    "v_ldexp_f32 v21, v21, s55\n"
    "v_max3_f32 v39, |v38|, |v21|, v39\n"
    "v_cvt_pk_bf16_f32 v21, v38, v21\n"
    "v_lshlrev_b32 v38, 16, v22\n"
    "v_and_b32 v22, 0xffff0000, v22\n"
    "v_ldexp_f32 v38, v38, s55\n"
    "v_ldexp_f32 v22, v22, s55\n"
    "v_max3_f32 v39, |v38|, |v22|, v39\n"
    "v_cvt_pk_bf16_f32 v22, v38, v22\n"
    "v_lshlrev_b32 v38, 16, v23\n"
    "v_and_b32 v23, 0xffff0000, v23\n"
    "v_ldexp_f32 v38, v38, s55\n"
    "v_ldexp_f32 v23, v23, s55\n"
    "v_max3_f32 v39, |v38|, |v23|, v39\n"
    "v_cvt_pk_bf16_f32 v23, v38, v23\n"
    "v_lshlrev_b32 v38, 16, v156\n"
    "v_and_b32 v156, 0xffff0000, v156\n"
    "v_ldexp_f32 v38, v38, s55\n"
    "v_ldexp_f32 v156, v156, s55\n"
    "v_max3_f32 v39, |v38|, |v156|, v39\n"
    "v_cvt_pk_bf16_f32 v156, v38, v156\n"
    "v_lshlrev_b32 v38, 16, v157\n"
    "v_and_b32 v157, 0xffff0000, v157\n"
    "v_ldexp_f32 v38, v38, s55\n"
    "v_ldexp_f32 v157, v157, s55\n"
    "v_max3_f32 v39, |v38|, |v157|, v39\n"
    "v_cvt_pk_bf16_f32 v157, v38, v157\n"
    "v_lshlrev_b32 v38, 16, v158\n"
    "v_and_b32 v158, 0xffff0000, v158\n"
    "v_ldexp_f32 v38, v38, s55\n"
    "v_ldexp_f32 v158, v158, s55\n"
    "v_max3_f32 v39, |v38|, |v158|, v39\n"
    "v_cvt_pk_bf16_f32 v158, v38, v158\n"
    "v_lshlrev_b32 v38, 16, v159\n"
    "v_and_b32 v159, 0xffff0000, v159\n"
    "v_ldexp_f32 v38, v38, s55\n"
    "v_ldexp_f32 v159, v159, s55\n"
    "v_max3_f32 v39, |v38|, |v159|, v39\n"
    "v_cvt_pk_bf16_f32 v159, v38, v159\n"
    "v_mov_b32 v38, 0\n"
    "ds_max_u32 v38, v39 offset:44600\n"
    "s_waitcnt lgkmcnt(0)\n"
    "s_branch Lsxr\n"
    "Lsy:\n"
    "s_waitcnt vmcnt(0)\n"
    "v_mov_b32 v39, 0\n"
    "v_lshlrev_b32 v38, 16, v8\n"
    "v_and_b32 v8, 0xffff0000, v8\n"
    "v_ldexp_f32 v38, v38, s55\n"
    "v_ldexp_f32 v8, v8, s55\n"
    "v_max3_f32 v39, |v38|, |v8|, v39\n"
    "v_cvt_pk_bf16_f32 v8, v38, v8\n"
    "v_lshlrev_b32 v38, 16, v9\n"
    "v_and_b32 v9, 0xffff0000, v9\n"
    "v_ldexp_f32 v38, v38, s55\n"
    "v_ldexp_f32 v9, v9, s55\n"
    "v_max3_f32 v39, |v38|, |v9|, v39\n"
    "v_cvt_pk_bf16_f32 v9, v38, v9\n"
    "v_lshlrev_b32 v38, 16, v10\n"
    "v_and_b32 v10, 0xffff0000, v10\n"
    "v_ldexp_f32 v38, v38, s55\n"
    "v_ldexp_f32 v10, v10, s55\n"
    "v_max3_f32 v39, |v38|, |v10|, v39\n"
    "v_cvt_pk_bf16_f32 v10, v38, v10\n"
    "v_lshlrev_b32 v38, 16, v11\n"
    "v_and_b32 v11, 0xffff0000, v11\n"
    "v_ldexp_f32 v38, v38, s55\n"
    "v_ldexp_f32 v11, v11, s55\n"
    "v_max3_f32 v39, |v38|, |v11|, v39\n"
    "v_cvt_pk_bf16_f32 v11, v38, v11\n"
    "v_lshlrev_b32 v38, 16, v12\n"
    "v_and_b32 v12, 0xffff0000, v12\n"
    "v_ldexp_f32 v38, v38, s55\n"
    "v_ldexp_f32 v12, v12, s55\n"
    "v_max3_f32 v39, |v38|, |v12|, v39\n"
    "v_cvt_pk_bf16_f32 v12, v38, v12\n"
    "v_lshlrev_b32 v38, 16, v13\n"
    "v_and_b32 v13, 0xffff0000, v13\n"
    "v_ldexp_f32 v38, v38, s55\n"
    "v_ldexp_f32 v13, v13, s55\n"
    "v_max3_f32 v39, |v38|, |v13|, v39\n"
    "v_cvt_pk_bf16_f32 v13, v38, v13\n"
    "v_lshlrev_b32 v38, 16, v14\n"
    "v_and_b32 v14, 0xffff0000, v14\n"
    "v_ldexp_f32 v38, v38, s55\n"
    "v_ldexp_f32 v14, v14, s55\n"
    "v_max3_f32 v39, |v38|, |v14|, v39\n"
    "v_cvt_pk_bf16_f32 v14, v38, v14\n"
    "v_lshlrev_b32 v38, 16, v15\n"
    "v_and_b32 v15, 0xffff0000, v15\n"
    "v_ldexp_f32 v38, v38, s55\n"
    "v_ldexp_f32 v15, v15, s55\n"
    "v_max3_f32 v39, |v38|, |v15|, v39\n"
    "v_cvt_pk_bf16_f32 v15, v38, v15\n"
    "v_lshlrev_b32 v38, 16, v16\n"
    "v_and_b32 v16, 0xffff0000, v16\n"
    "v_ldexp_f32 v38, v38, s55\n"
    "v_ldexp_f32 v16, v16, s55\n"
    "v_max3_f32 v39, |v38|, |v16|, v39\n"
    "v_cvt_pk_bf16_f32 v16, v38, v16\n"
    "v_lshlrev_b32 v38, 16, v17\n"
    "v_and_b32 v17, 0xffff0000, v17\n"
    "v_ldexp_f32 v38, v38, s55\n"
    "v_ldexp_f32 v17, v17, s55\n"
    "v_max3_f32 v39, |v38|, |v17|, v39\n"
    "v_cvt_pk_bf16_f32 v17, v38, v17\n"
    "v_lshlrev_b32 v38, 16, v18\n"
    "v_and_b32 v18, 0xffff0000, v18\n"
    "v_ldexp_f32 v38, v38, s55\n"
    "v_ldexp_f32 v18, v18, s55\n"
    "v_max3_f32 v39, |v38|, |v18|, v39\n"
    "v_cvt_pk_bf16_f32 v18, v38, v18\n"
    "v_lshlrev_b32 v38, 16, v19\n"
    "v_and_b32 v19, 0xffff0000, v19\n"
    "v_ldexp_f32 v38, v38, s55\n"
    "v_ldexp_f32 v19, v19, s55\n"
    "v_max3_f32 v39, |v38|, |v19|, v39\n"
    "v_cvt_pk_bf16_f32 v19, v38, v19\n"
    "v_mov_b32 v38, 0\n"
    "ds_max_u32 v38, v39 offset:44600\n"
    "s_waitcnt lgkmcnt(0)\n"
    "s_branch Lsyr\n"
    "Lend:\n"
    :: "s"(__builtin_amdgcn_kernarg_segment_ptr()), "s"(blockIdx.x), "s"(KTAB), "v"(threadIdx.x), "s"(lds)
    : "v1","v2","v3","v4","v5","v6","v7","v8","v9","v10","v11","v12","v13","v14","v15","v16","v17","v18","v19","v20","v21","v22","v23","v24","v25","v26","v27","v28","v29","v30","v31","v32","v33","v34","v35","v36","v37","v38","v39","v40","v41","v42","v43","v44","v45","v46","v47","v48","v49","v50","v51","v52","v53","v54","v55","v56","v57","v58","v59","v60","v61","v62","v63","v64","v65","v66","v67","v68","v69","v70","v71","v72","v73","v74","v75","v76","v77","v78","v79","v80","v81","v82","v83","v84","v85","v86","v87","v88","v89","v90","v91","v92","v93","v94","v95","v96","v97","v98","v99","v100","v101","v102","v103","v104","v105","v106","v107","v108","v109","v110","v111","v112","v113","v114","v115","v116","v117","v118","v119","v120","v121","v122","v123","v124","v125","v126","v127","v128","v129","v130","v131","v132","v133","v134","v135","v136","v137","v138","v139","v140","v141","v142","v143","v144","v145","v146","v147","v148","v149","v150","v151","v152","v153","v154","v155","v156","v157","v158","v159","v160","v161","v162","v163","v164","v165","v166","v167","v168","v169","v170","v171","v172","v173","v174","v175","v176","v177","v178","v179","v180","v181","v182","v183","v184","v185","v186","v187","v188","v189","v190","v191","v192","v193","v194","v195","v196","v197","v198","v199","v200","v201","v202","v203","v204","v205","v206","v207","v208","v209","v210","v211","v212","v213","v214","v215","v216","v217","v218","v219","v220","v221","v222","v223","v224","v225","v226","v227","v228","v229","v230","v231","v232","v233","v234","v235","v236","v237","v238","v239","v240","v241","v242","v243","v244","v245","v246","v247","v248","v249","v250","v251","v252","v253","v254","v255","s8","s9","s10","s11","s12","s13","s14","s15","s16","s17","s18","s19","s20","s21","s22","s23","s24","s25","s26","s27","s28","s29","s30","s31","s34","s35","s36","s37","s38","s39","s40","s41","s42","s43","s44","s45","s46","s47","s48","s49","s50","s51","s52","s53","s54","s55","s56","s57","s58","s59","s60","s61","s62","s63","s64","s65","s66","s67","s68","s69","s70","s71","s72","s73","s74","s75","s76","s77","s78","s79","s80","s81","s82","s83","s84","s85","s86","s87","s88","s89","s90","s91","s92","s93","s94","s95","s96","s97","s98","s99","vcc","scc","memory");
}

extern "C" void kernel_launch(void* const* d_in, const int* in_sizes, int n_in, void* d_out, int out_size,
                              void* d_ws, size_t ws_size, hipStream_t stream) {
  xdeepfm_kernel<<<out_size / 64, 512, 0, stream>>>(d_in[0], d_in[1], d_in[2], d_in[3], d_in[4], d_in[5], d_in[6], d_in[7], d_out, d_ws);
}
```
